```python
import torch

def _softmax(x, axis=-1):
    return torch.softmax(x, dim=axis)
def _gelu(x):
    return torch.nn.functional.gelu(x, approximate="tanh")
def _silu(x):
    return torch.nn.functional.silu(x)
def _sigmoid(x):
    return torch.sigmoid(x)
def _layernorm(x, axis=-1, eps=1e-5):
    m = x.mean(dim=axis, keepdim=True)
    v = x.var(dim=axis, keepdim=True, unbiased=False)
    return (x - m) / torch.sqrt(v + eps)
def _scatter_add(tgt, idx, src, axis=0):
    return tgt.clone().index_add_(axis, idx, src)
def _segment_sum(src, seg, n):
    out = torch.zeros((n,) + src.shape[1:], dtype=src.dtype)
    return out.index_add_(0, seg, src)
def _pad(x, pw):
    flat = tuple(v for pair in reversed(pw) for v in pair)
    return torch.nn.functional.pad(x, flat)

kernel_name = 'sequential_preLN__softmax_full_mha__mixer_token_mlp__prompt'

def setup_inputs():

    _w = (lambda a: (lambda t: t.to(torch.bfloat16) if t.is_floating_point() else t)(torch.as_tensor(a)))
    def _A_setup():
        B, S, D = 1, 2048, 576
        return {
            "x": _w((lambda *s: torch.randn(*s, dtype=torch.bfloat16) * 0.05)(B, S, D)),
            "wqkv": _w((lambda *s: torch.randn(*s, dtype=torch.bfloat16) * 0.05)(D, 3 * D)),
            "wo": _w((lambda *s: torch.randn(*s, dtype=torch.bfloat16) * 0.05)(D, D)),
        }
    _A_in = _A_setup()

    def _B_setup():
        B, S, D, F = 1, 2048, 576, 4608
        return {
            "x": _w((lambda *s: torch.randn(*s, dtype=torch.bfloat16) * 0.05)(B, S, D)),
            "w1": _w((lambda *s: torch.randn(*s, dtype=torch.bfloat16) * 0.8)(S, F) * (S ** -0.5)),
            "b1": _w((lambda *s: torch.randn(*s, dtype=torch.bfloat16) * 12.8)(F) * 0.02),
            "w2": _w((lambda *s: torch.randn(*s, dtype=torch.bfloat16) * 0.4)(F, S) * (F ** -0.5)),
            "b2": _w((lambda *s: torch.randn(*s, dtype=torch.bfloat16) * 6.4)(S) * 0.02),
        }
    _B_in = _B_setup()

    return {
        "A_x": _A_in["x"],
        "A_wqkv": _A_in["wqkv"],
        "A_wo": _A_in["wo"],
        "B_x": _B_in["x"],
        "B_w1": _B_in["w1"],
        "B_b1": _B_in["b1"],
        "B_w2": _B_in["w2"],
        "B_b2": _B_in["b2"],
    }

def reference(A_x, A_wqkv, A_wo, B_x, B_w1, B_b1, B_w2, B_b2):
    def _A_ref(x, wqkv, wo):
        B, S, D = x.shape
        H = 32; Dh = D // H
        qkv = (lambda x, *d: x.permute(*d))((x @ wqkv).reshape(B, S, 3, H, Dh), 2, 0, 3, 1, 4)
        q, k, v = qkv[0], qkv[1], qkv[2]
        scale = Dh ** -0.5
        scores = torch.einsum("bhqd,bhkd->bhqk", q, k) * scale
        p = _softmax(scores, axis=-1)
        o = torch.einsum("bhqk,bhkd->bhqd", p, v)
        return (lambda x, *d: x.permute(*d))(o, 0, 2, 1, 3).reshape(B, S, D) @ wo

    def _B_ref(x, w1, b1, w2, b2):
        B, S, D = x.shape
        h = _layernorm(x, axis=-1)
        u = torch.einsum("bsd,sf->bdf", h, w1) + b1
        u = _gelu(u)
        y = torch.einsum("bdf,fs->bds", u, w2) + b2
        return (lambda x, *d: x.permute(*d))(y, 0, 2, 1)

    x = A_x
    h = x + _A_ref(_layernorm(x, axis=-1), A_wqkv, A_wo)
    h2 = h + _B_ref(_layernorm(h, axis=-1), B_w1, B_b1, B_w2, B_b2)
    return h2
```

```cpp
#include <hip/hip_runtime.h>
#include <stdint.h>

typedef short    bf16x8 __attribute__((ext_vector_type(8)));
typedef short    s16x4  __attribute__((ext_vector_type(4)));
typedef float    f32x4  __attribute__((ext_vector_type(4)));
typedef float    f32x16 __attribute__((ext_vector_type(16)));
typedef unsigned u32x2  __attribute__((ext_vector_type(2)));
typedef unsigned u32x4  __attribute__((ext_vector_type(4)));
typedef __bf16   bf16x2_t __attribute__((ext_vector_type(2)));
typedef unsigned short u16;
#define LDS_AS __attribute__((address_space(3)))
#define GLB_AS __attribute__((address_space(1)))
typedef LDS_AS const char* lds_cptr;
typedef GLB_AS unsigned gu32;
typedef GLB_AS unsigned long long gu64;
#define RLX_AGENT __ATOMIC_RELAXED, __HIP_MEMORY_SCOPE_AGENT

constexpr int S_ = 2048, D_ = 576, F_ = 4608;
constexpr float LN_EPS = 1e-5f;
constexpr float QSCALE = 0.23570226039551584f * 1.4426950408889634f;

constexpr unsigned VREC    = 656;
constexpr unsigned KVHEAD  = 65536 + 128 * VREC;
constexpr size_t WS_QM   = 0;
constexpr size_t WS_QX   = WS_QM + 32u * 65536;
constexpr size_t WS_KV   = WS_QX + 32u * 8192;
constexpr size_t WS_OF   = WS_KV + 32u * KVHEAD;
constexpr size_t WS_WOF  = WS_OF + 128u * 18 * 1024;
constexpr size_t WS_HN   = WS_WOF + 36u * 18 * 1024;
constexpr size_t WS_H    = WS_HN + 36u * 64 * 1024;
constexpr size_t WS_U    = WS_H + 2048u * 576 * 4;
constexpr size_t WS_GRAN = WS_U + 36u * 144 * 1024;
constexpr size_t WS_CNT  = WS_GRAN + 64u * 12 * 512;
constexpr size_t WS_ZEND = WS_CNT + 64u * 256;
constexpr size_t WS_SLAB = WS_ZEND;

__device__ __forceinline__ unsigned cvtpk(float lo, float hi) {
  typedef float f32x2_t __attribute__((ext_vector_type(2)));
  return __builtin_bit_cast(unsigned, __builtin_convertvector((f32x2_t){lo, hi}, bf16x2_t));
}
__device__ __forceinline__ float bf2f(u16 v) { return __uint_as_float((unsigned)v << 16); }
__device__ __forceinline__ void glds16(const void* sbase, unsigned voff, unsigned lds_base) {
  unsigned sv;
  asm volatile("s_mov_b32 %0, m0\n\ts_mov_b32 m0, %3\n\ts_nop 0\n\tglobal_load_lds_dwordx4 %1, %2\n\ts_mov_b32 m0, %0"
               : "=&s"(sv) : "v"(voff), "s"(sbase), "s"(lds_base) : "memory");
}
__device__ __forceinline__ s16x4 trread(lds_cptr p) {
  typedef short v4i16_t __attribute__((ext_vector_type(4)));
  return __builtin_bit_cast(s16x4, __builtin_amdgcn_ds_read_tr16_b64_v4i16((LDS_AS v4i16_t*)p));
}
__device__ __forceinline__ bf16x8 cat8(s16x4 a, s16x4 b) {
  return (bf16x8){a[0], a[1], a[2], a[3], b[0], b[1], b[2], b[3]};
}
__device__ __forceinline__ bf16x8 ldsr128(lds_cptr p) { return *(const LDS_AS bf16x8*)p; }
#define MFMA16(a, b, c) __builtin_amdgcn_mfma_f32_16x16x32_bf16((a), (b), (c), 0, 0, 0)
#define MFMA32(a, b, c) __builtin_amdgcn_mfma_f32_32x32x16_bf16((a), (b), (c), 0, 0, 0)
#define WAITV(n) asm volatile("s_waitcnt vmcnt(" #n ")" ::: "memory")
#define BARRIER() asm volatile("s_waitcnt lgkmcnt(0)\n\ts_barrier" ::: "memory")
__device__ __forceinline__ float dot2(unsigned a, unsigned b, float c) {
  return __builtin_amdgcn_fdot2_f32_bf16(__builtin_bit_cast(bf16x2_t, a), __builtin_bit_cast(bf16x2_t, b), c, false);
}
constexpr unsigned ONES2 = 0x3F803F80u;

__global__ __launch_bounds__(256, 1) void k1_qkv(const u16* __restrict__ x, const u16* __restrict__ wqkv, char* __restrict__ ws) {
  __shared__ __attribute__((aligned(16))) char lds[131072 + 512];
  const int tid = threadIdx.x, lane = tid & 63;
  const int w = __builtin_amdgcn_readfirstlane(tid >> 6);
  const int b = blockIdx.x, xg = b & 7, o = b >> 3;
  const int bm = (xg >> 1) * 4 + (o & 3), hg = (xg & 1) * 8 + (o >> 2);
  const unsigned lds0 = (unsigned)(uintptr_t)lds;
  const int g = lane >> 4, c16 = lane & 15;

  unsigned woff[7];
#pragma unroll
  for (int i = 0; i < 7; ++i) {
    unsigned c = (unsigned)((4 * i + w) * 64 + lane);
    unsigned pos = c / 14u, ch = c - pos * 14u;
    unsigned k = (pos & ~12u) | ((pos & 4u) << 1) | ((pos & 8u) >> 1);
    woff[i] = k * 3456u + 216u * hg + (ch < 13u ? 16u * ch : 200u);
  }
  const char* xp0 = (const char*)x + (size_t)(bm * 128 + w * 32 + c16) * 1152 + g * 16;
  const char* xp1 = xp0 + 16 * 1152;
  u32x4 xf[18][2];
#define XLOAD(ks) do { \
    asm volatile("global_load_dwordx4 %0, %1, off offset:%2" : "=v"(xf[ks][0]) : "v"(xp0), "i"((ks) * 64) : "memory"); \
    asm volatile("global_load_dwordx4 %0, %1, off offset:%2" : "=v"(xf[ks][1]) : "v"(xp1), "i"((ks) * 64) : "memory"); } while (0)
#define WLOAD(i) do { const int n_ = 4 * (i) + w; \
    const char* sb_ = (const char*)wqkv + ((n_ < 126) ? (size_t)((i) / 7) * 442368u : (size_t)0); \
    glds16(sb_, woff[(i) % 7], lds0 + (unsigned)n_ * 1024u); } while (0)
  XLOAD(0); XLOAD(1); WLOAD(0); WLOAD(1); WLOAD(2); WLOAD(3); XLOAD(2); XLOAD(3); WLOAD(4); WLOAD(5); WLOAD(6); WLOAD(7);
  XLOAD(4); XLOAD(5); XLOAD(6); XLOAD(7); XLOAD(8);
  WLOAD(8); WLOAD(9); WLOAD(10); WLOAD(11); WLOAD(12); WLOAD(13); WLOAD(14); WLOAD(15);
  XLOAD(9); XLOAD(10); XLOAD(11); XLOAD(12);
  WLOAD(16); WLOAD(17); WLOAD(18); WLOAD(19); WLOAD(20); WLOAD(21); WLOAD(22); WLOAD(23);
  XLOAD(13); XLOAD(14); XLOAD(15); XLOAD(16); XLOAD(17);
  WLOAD(24); WLOAD(25); WLOAD(26); WLOAD(27); WLOAD(28); WLOAD(29); WLOAD(30); WLOAD(31);

  const int q4 = (lane & 15) >> 2, p4 = lane & 3;
  const lds_cptr wb  = (lds_cptr)lds + (((g >> 1) * 16 + (g & 1) * 4 + q4) * 224 + 8 * p4);
  const lds_cptr wb6 = (lds_cptr)lds + (((g >> 1) * 16 + (g & 1) * 4 + q4) * 224 + 192 + 8 * (p4 ^ (p4 >> 1)));
  f32x4 acc[7][2];
#pragma unroll
  for (int n = 0; n < 7; ++n) { acc[n][0] = (f32x4){0, 0, 0, 0}; acc[n][1] = (f32x4){0, 0, 0, 0}; }
  float sx0 = 0.f, sx1 = 0.f, sq0 = 0.f, sq1 = 0.f, csA = 0.f, csB = 0.f;

#define XWAIT(N, a, b_, c_, d_) asm volatile("s_waitcnt vmcnt(" #N ")\n\ts_barrier" : "+v"(xf[a][0]), "+v"(xf[a][1]), "+v"(xf[b_][0]), "+v"(xf[b_][1]), \
    "+v"(xf[c_][0]), "+v"(xf[c_][1]), "+v"(xf[d_][0]), "+v"(xf[d_][1]) :: "memory")
#define XPIN(a) asm volatile("" : "+v"(xf[a][0]), "+v"(xf[a][1]) :: "memory")
#define KSTEP(ks) do { \
    const bf16x8 x0_ = __builtin_bit_cast(bf16x8, xf[ks][0]), x1_ = __builtin_bit_cast(bf16x8, xf[ks][1]); \
    _Pragma("unroll") for (int n = 0; n < 7; ++n) { \
      const lds_cptr pp_ = (n < 6 ? wb + n * 32 : wb6) + (ks) * 7168; \
      const bf16x8 wf_ = cat8(trread(pp_), trread(pp_ + 1792)); \
      acc[n][0] = MFMA16(wf_, x0_, acc[n][0]); acc[n][1] = MFMA16(wf_, x1_, acc[n][1]); \
      if ((n & 3) == w) { const u32x4 wu_ = __builtin_bit_cast(u32x4, wf_); \
        float t_ = (n < 4) ? csA : csB; \
        t_ = dot2(wu_[0], ONES2, t_); t_ = dot2(wu_[1], ONES2, t_); t_ = dot2(wu_[2], ONES2, t_); t_ = dot2(wu_[3], ONES2, t_); \
        if (n < 4) csA = t_; else csB = t_; } \
    } \
    _Pragma("unroll") for (int j = 0; j < 4; ++j) { \
      sx0 = dot2(xf[ks][0][j], ONES2, sx0); sq0 = dot2(xf[ks][0][j], xf[ks][0][j], sq0); \
      sx1 = dot2(xf[ks][1][j], ONES2, sx1); sq1 = dot2(xf[ks][1][j], xf[ks][1][j], sq1); } \
  } while (0)

  XWAIT(52, 0, 1, 2, 3);
  KSTEP(0); KSTEP(1); KSTEP(2); KSTEP(3);
  XWAIT(34, 4, 5, 6, 7); XPIN(8);
  KSTEP(4); KSTEP(5); KSTEP(6); KSTEP(7); KSTEP(8);
  XWAIT(18, 9, 10, 11, 12);
  KSTEP(9); KSTEP(10); KSTEP(11); KSTEP(12);
  XWAIT(0, 13, 14, 15, 16); XPIN(17);
  KSTEP(13); KSTEP(14); KSTEP(15); KSTEP(16); KSTEP(17);

  if (tid < 100) *(u32x4*)(ws + WS_GRAN + (size_t)b * 1600 + tid * 16) = (u32x4){0, 0, 0, 0};

  csA += __shfl_xor(csA, 16); csA += __shfl_xor(csA, 32);
  csB += __shfl_xor(csB, 16); csB += __shfl_xor(csB, 32);
  float* csl = (float*)(lds + 131072);
  if (lane < 16) { csl[w * 16 + lane] = csA; if (w < 3) csl[(w + 4) * 16 + lane] = csB; }
  sx0 += __shfl_xor(sx0, 16); sx0 += __shfl_xor(sx0, 32); sq0 += __shfl_xor(sq0, 16); sq0 += __shfl_xor(sq0, 32);
  sx1 += __shfl_xor(sx1, 16); sx1 += __shfl_xor(sx1, 32); sq1 += __shfl_xor(sq1, 16); sq1 += __shfl_xor(sq1, 32);
  __syncthreads();
  float mean[2], rstd[2];
  mean[0] = sx0 * (1.f / 576.f); mean[1] = sx1 * (1.f / 576.f);
  rstd[0] = rsqrtf(fmaxf(sq0 * (1.f / 576.f) - mean[0] * mean[0], 0.f) + LN_EPS);
  rstd[1] = rsqrtf(fmaxf(sq1 * (1.f / 576.f) - mean[1] * mean[1], 0.f) + LN_EPS);

#pragma unroll
  for (int tt = 0; tt < 2; ++tt) {
    const unsigned T = (unsigned)(bm * 128 + w * 32 + tt * 16 + c16);
    const unsigned tQK = (T >> 5) * 1024u + (T & 31u) * 16u;
    const unsigned tV  = 65536u + (T >> 4) * VREC + (T & 15u) * 32u;
    const unsigned tX  = 65536u + (T >> 4) * VREC + 512u + (T & 15u) * 8u;
    const unsigned tQX = T * 4u;
#pragma unroll
    for (int n = 0; n < 7; ++n) {
      const f32x4 cs4 = *(const f32x4*)(csl + n * 16 + g * 4);
#pragma unroll
      for (int e = 0; e < 2; ++e) {
        const unsigned col = (unsigned)(n * 16 + g * 4 + e * 2);
        const unsigned hl = col / 18u, dp = (col - hl * 18u) >> 1;
        const unsigned hs = (unsigned)hg * 6u + hl, which = hs >> 5, h = hs & 31u;
        float v0 = (acc[n][tt][2 * e] - mean[tt] * cs4[2 * e]) * rstd[tt];
        float v1 = (acc[n][tt][2 * e + 1] - mean[tt] * cs4[2 * e + 1]) * rstd[tt];
        if (which == 0) { v0 *= QSCALE; v1 *= QSCALE; }
        unsigned off;
        if (which == 0) off = (dp < 8u) ? (unsigned)WS_QM + h * 65536u + tQK + (dp >> 2) * 512u + (dp & 3u) * 4u
                                        : (unsigned)WS_QX + h * 8192u + tQX;
        else if (which == 1) off = (unsigned)WS_KV + h * KVHEAD + ((dp < 8u) ? tQK + (dp >> 2) * 512u + (dp & 3u) * 4u : tX);
        else off = (unsigned)WS_KV + h * KVHEAD + ((dp < 8u) ? tV + dp * 4u : tX + 4u);
        if (col < 108u) {
          *(unsigned*)(ws + off) = cvtpk(v0, v1);
          if (which == 2 && dp == 8u && (T & 15u) == 0u)
            *(u32x2*)(ws + off + 124u) = (u32x2){ONES2, ONES2};
        }
      }
    }
  }
#undef XLOAD
#undef WLOAD
#undef XWAIT
#undef XPIN
#undef KSTEP
}

constexpr unsigned LQ = KVHEAD;
#define PF_W1 5
#define PF_W2 5
__global__ __launch_bounds__(1024, 1) void k2_attn(char* __restrict__ ws, const u16* __restrict__ wo,
                                                    const u16* __restrict__ w1, const u16* __restrict__ w2) {
  __shared__ __attribute__((aligned(16))) char lds[KVHEAD + 9216];
  const int tid = threadIdx.x, lane = tid & 63;
  const int w = __builtin_amdgcn_readfirstlane(tid >> 6);
  const int b = blockIdx.x, xg = b & 7;
  const int h = xg * 4 + ((b >> 3) & 3), qt = b >> 5;
  const int qg = w & 7, half = w >> 3;
  const unsigned lds0 = (unsigned)(uintptr_t)lds;
  const int r = lane & 31, hh = lane >> 5;
  const char* kvg = ws + WS_KV + (size_t)h * KVHEAD;
  const unsigned l16 = (unsigned)lane * 16u;
#pragma unroll
  for (int i = 0; i < 3; ++i) {
    const int s = i * 16 + w;
    if (s < 8) glds16(ws + WS_QM + (size_t)h * 65536 + (size_t)(qt * 8 + s) * 1024, l16, lds0 + LQ + s * 1024);
    else if (s == 8) glds16(ws + WS_QX + (size_t)h * 8192 + (size_t)qt * 1024, l16, lds0 + LQ + 8192);
    else {
      const int p = (s < 17) ? s - 9 : (s < 25) ? 32 + s - 17 : (s < 36) ? 64 + s - 25 : (s < 47) ? 105 + s - 36 : 8;
      glds16(kvg + (size_t)p * 1024, l16, lds0 + p * 1024);
    }
  }
#pragma unroll
  for (int i = 0; i < 7; ++i) {
    const int s = i * 16 + w;
    const int p = (s < 23) ? 9 + s : (s < 47) ? 40 + s - 23 : (s < 77) ? 75 + s - 47 : (s < 107) ? 116 + s - 77 : 145;
    glds16(kvg + (size_t)p * 1024, l16, lds0 + p * 1024);
  }
  const int gq = lane >> 4, q4 = (lane & 15) >> 2, p4 = lane & 3;
  const unsigned vrec0 = 65536u + (unsigned)half * 64u * VREC;
  unsigned va0, va1;
  if ((gq & 1) == 0) { va0 = vrec0 + (4 * hh + q4) * 32 + 8 * p4; va1 = va0 + 256; }
  else if (p4 == 0)  { va0 = vrec0 + 512 + (4 * hh + q4) * 8; va1 = va0 + 64; }
  else               { va0 = vrec0 + 640; va1 = va0; }
  const lds_cptr vp0 = (lds_cptr)lds + va0, vp1 = (lds_cptr)lds + va1;
  const lds_cptr kp = (lds_cptr)lds + (half * 32768 + hh * 512 + r * 16);
  const lds_cptr xp = (lds_cptr)lds + (vrec0 + (unsigned)(2 * hh) * 2u * VREC + (unsigned)(r >> 4) * VREC + 512u + (unsigned)(r & 15) * 8u);

  asm volatile("s_waitcnt vmcnt(7)\n\ts_barrier" ::: "memory");
  const bf16x8 qm = ldsr128((lds_cptr)lds + (LQ + qg * 1024 + hh * 512 + r * 16));
  const unsigned qxd = *(const LDS_AS unsigned*)((lds_cptr)lds + (LQ + 8192 + (qg * 32 + r) * 4));
  bf16x8 qx[4];
  {
    const unsigned qa = hh == 0 ? qxd : 0u, qb = hh == 1 ? qxd : 0u;
    qx[0] = __builtin_bit_cast(bf16x8, (u32x4){qa, 0u, 0u, 0u});
    qx[1] = __builtin_bit_cast(bf16x8, (u32x4){0u, 0u, qa, 0u});
    qx[2] = __builtin_bit_cast(bf16x8, (u32x4){qb, 0u, 0u, 0u});
    qx[3] = __builtin_bit_cast(bf16x8, (u32x4){0u, 0u, qb, 0u});
  }
  f32x16 oacc = {};
  const f32x16 zero16 = {};
#define TILE(it, t) do { \
    const bf16x8 kf_ = ldsr128(kp + ((it) * 4 + (t)) * 1024); \
    f32x16 s_ = MFMA32(kf_, qm, zero16); \
    s_ = MFMA32(kx_, qx[t], s_); \
    const int ro_ = ((it) * 8 + (t) * 2) * (int)VREC; \
    const bf16x8 v0_ = cat8(trread(vp0 + ro_), trread(vp1 + ro_)); \
    const bf16x8 v1_ = cat8(trread(vp0 + ro_ + VREC), trread(vp1 + ro_ + VREC)); \
    u32x4 pa_, pb_; \
    _Pragma("unroll") for (int i = 0; i < 4; ++i) { \
      pa_[i] = cvtpk(__builtin_amdgcn_exp2f(s_[2 * i]), __builtin_amdgcn_exp2f(s_[2 * i + 1])); \
      pb_[i] = cvtpk(__builtin_amdgcn_exp2f(s_[8 + 2 * i]), __builtin_amdgcn_exp2f(s_[8 + 2 * i + 1])); } \
    oacc = MFMA32(v0_, __builtin_bit_cast(bf16x8, pa_), oacc); \
    oacc = MFMA32(v1_, __builtin_bit_cast(bf16x8, pb_), oacc); \
  } while (0)
#define ITER(it) do { \
    const u32x2 xa_ = *(const LDS_AS u32x2*)(xp + (it) * 8 * (int)VREC), xb_ = *(const LDS_AS u32x2*)(xp + (it) * 8 * (int)VREC + 2 * (int)VREC); \
    const bf16x8 kx_ = __builtin_bit_cast(bf16x8, (u32x4){xa_[0], xa_[1], xb_[0], xb_[1]}); \
    TILE(it, 0); TILE(it, 1); TILE(it, 2); TILE(it, 3); } while (0)
  ITER(0); ITER(1);
  asm volatile("s_waitcnt vmcnt(0)\n\ts_barrier" ::: "memory");

  u16 wog[8];
  const int wtask = b * 162 + (tid - 512);
  const bool wact = (tid >= 512) && (tid < 512 + 162);
  {
    const int tk = wact ? wtask : 0;
    const int l_ = tk & 63, ks = (tk >> 6) % 18, nt = tk / (64 * 18);
    const int g_ = l_ >> 4, n_ = nt * 16 + (l_ & 15);
#pragma unroll
    for (int j = 0; j < 8; ++j) {
      const int k = (ks < 16) ? 18 * (2 * ks + (g_ >> 1)) + 4 * (g_ & 1) + (j & 3) + 8 * (j >> 2)
                              : 18 * (16 * (ks - 16) + 4 * g_ + (j >> 1)) + 16 + (j & 1);
      wog[j] = (w >= 8 && w <= 10) ? wo[k * 576 + n_] : (u16)0;
    }
  }
  u32x4 sink = {0, 0, 0, 0};
  const int gw = (b >> 3) * 16 + w;
#define PFETCH(j) do { \
    if ((j) < PF_W2) { int L_ = (j) * 512 + gw; if (L_ >= 2304) L_ = gw; const unsigned ch_ = (unsigned)L_ * 64u + lane; \
      const char* a_ = (const char*)w2 + (size_t)(ch_ >> 5) * 4096 + xg * 512 + (ch_ & 31u) * 16; \
      asm volatile("global_load_dwordx4 %0, %1, off" : "+v"(sink) : "v"(a_)); } \
    else if ((j) - PF_W2 < PF_W1) { int L_ = ((j) - PF_W2) * 512 + gw; if (L_ >= 2304) L_ = gw; const unsigned ch_ = (unsigned)L_ * 64u + lane; \
      const unsigned rw_ = ch_ / 72u; \
      const char* a_ = (const char*)w1 + (size_t)rw_ * 9216 + xg * 1152 + (ch_ - rw_ * 72u) * 16; \
      asm volatile("global_load_dwordx4 %0, %1, off" : "+v"(sink) : "v"(a_)); } \
  } while (0)
  PFETCH(0); PFETCH(1); ITER(2);
  PFETCH(2); PFETCH(3); ITER(3);
  PFETCH(4); PFETCH(5); ITER(4);
  PFETCH(6); PFETCH(7); ITER(5);
  PFETCH(8); PFETCH(9); ITER(6);
  ITER(7);

  float mref = 0.f;
  {
    const float lsum = oacc[12];
    bool bad = !(lsum >= 8.271806125530277e-25f && lsum <= 1.2676506002282294e30f);
#pragma unroll
    for (int i = 0; i < 8; ++i) bad |= !(__builtin_fabsf(oacc[i]) <= 3.0e38f);
    if (hh == 0) bad |= !(__builtin_fabsf(oacc[10]) <= 3.0e38f) || !(__builtin_fabsf(oacc[11]) <= 3.0e38f);
    if (__any(bad)) {
      oacc = zero16;
      float m = -INFINITY;
      for (int it = 0; it < 8; ++it) {
        const u32x2 xa_ = *(const LDS_AS u32x2*)(xp + it * 8 * (int)VREC), xb_ = *(const LDS_AS u32x2*)(xp + it * 8 * (int)VREC + 2 * (int)VREC);
        const bf16x8 kx_ = __builtin_bit_cast(bf16x8, (u32x4){xa_[0], xa_[1], xb_[0], xb_[1]});
#pragma unroll
        for (int t = 0; t < 4; ++t) {
          const bf16x8 kf_ = ldsr128(kp + (it * 4 + t) * 1024);
          f32x16 s_ = MFMA32(kf_, qm, zero16);
          s_ = MFMA32(kx_, qx[t], s_);
          float tm = s_[0];
#pragma unroll
          for (int i = 1; i < 16; ++i) tm = fmaxf(tm, s_[i]);
          tm = fmaxf(tm, __shfl_xor(tm, 32));
          const float mn = fmaxf(m, tm);
          const float sc = __builtin_amdgcn_exp2f(m - mn);
          m = mn;
#pragma unroll
          for (int i = 0; i < 16; ++i) oacc[i] *= sc;
          const int ro_ = (it * 8 + t * 2) * (int)VREC;
          const bf16x8 v0_ = cat8(trread(vp0 + ro_), trread(vp1 + ro_));
          const bf16x8 v1_ = cat8(trread(vp0 + ro_ + VREC), trread(vp1 + ro_ + VREC));
          u32x4 pa_, pb_;
#pragma unroll
          for (int i = 0; i < 4; ++i) {
            pa_[i] = cvtpk(__builtin_amdgcn_exp2f(s_[2 * i] - m), __builtin_amdgcn_exp2f(s_[2 * i + 1] - m));
            pb_[i] = cvtpk(__builtin_amdgcn_exp2f(s_[8 + 2 * i] - m), __builtin_amdgcn_exp2f(s_[8 + 2 * i + 1] - m));
          }
          oacc = MFMA32(v0_, __builtin_bit_cast(bf16x8, pa_), oacc);
          oacc = MFMA32(v1_, __builtin_bit_cast(bf16x8, pb_), oacc);
        }
      }
      mref = m;
    }
  }
  __syncthreads();
  f32x4* mb = (f32x4*)(lds + qg * 4096);
  if (half == 1) {
    mb[lane] = (f32x4){oacc[0], oacc[1], oacc[2], oacc[3]};
    mb[64 + lane] = (f32x4){oacc[4], oacc[5], oacc[6], oacc[7]};
    mb[128 + lane] = (f32x4){oacc[10], oacc[11], oacc[12], mref};
  }
  __syncthreads();
  if (half == 0) {
    const f32x4 a0 = mb[lane], a1 = mb[64 + lane], a2 = mb[128 + lane];
    const float M = fmaxf(mref, a2[3]);
    const float s0 = __builtin_amdgcn_exp2f(mref - M), s1 = __builtin_amdgcn_exp2f(a2[3] - M);
    const float inv = 1.0f / (oacc[12] * s0 + a2[2] * s1);
    const float c0 = s0 * inv, c1 = s1 * inv;
    u32x4 ov;
    ov[0] = cvtpk(oacc[0] * c0 + a0[0] * c1, oacc[1] * c0 + a0[1] * c1);
    ov[1] = cvtpk(oacc[2] * c0 + a0[2] * c1, oacc[3] * c0 + a0[3] * c1);
    ov[2] = cvtpk(oacc[4] * c0 + a1[0] * c1, oacc[5] * c0 + a1[1] * c1);
    ov[3] = cvtpk(oacc[6] * c0 + a1[2] * c1, oacc[7] * c0 + a1[3] * c1);
    const int T = qt * 256 + qg * 32 + r;
    char* ofb = ws + WS_OF + (size_t)(T >> 4) * 18432;
    *(u32x4*)(ofb + (h >> 1) * 1024 + (((h & 1) * 2 + hh) * 16 + (T & 15)) * 16) = ov;
    if (hh == 0)
      *(unsigned*)(ofb + (16 + (h >> 4)) * 1024 + (((h & 15) >> 2) * 16 + (T & 15)) * 16 + (h & 3) * 4) =
          cvtpk(oacc[10] * c0 + a2[0] * c1, oacc[11] * c0 + a2[1] * c1);
  } else if (wact) {
    u32x4 v;
#pragma unroll
    for (int j = 0; j < 4; ++j) v[j] = (unsigned)wog[2 * j] | ((unsigned)wog[2 * j + 1] << 16);
    *(u32x4*)(ws + WS_WOF + (size_t)wtask * 16) = v;
  }
  asm volatile("s_waitcnt vmcnt(0)" : "+v"(sink) :: "memory");
#undef TILE
#undef ITER
#undef PFETCH
}

__global__ __launch_bounds__(192, 1) void k3_proj(const u16* __restrict__ x, char* __restrict__ ws) {
  const int tid = threadIdx.x, lane = tid & 63;
  const int w = __builtin_amdgcn_readfirstlane(tid >> 6);
  const int b = blockIdx.x, xg = b & 7, o = b >> 3;
  const int tt32 = xg * 8 + (o >> 2), ct = o & 3;
  const int g = lane >> 4, c16 = lane & 15;
  const int nt0 = ct * 9 + w * 3;
  const char* ofp = ws + WS_OF + (size_t)(tt32 * 2) * 18432 + lane * 16;
  const char* wfp = ws + WS_WOF + (size_t)nt0 * 18432 + lane * 16;
  f32x4 acc[2][3];
#pragma unroll
  for (int t = 0; t < 2; ++t)
#pragma unroll
    for (int n = 0; n < 3; ++n)
#pragma unroll
      for (int j = 0; j < 4; ++j)
        acc[t][n][j] = bf2f(x[(size_t)(tt32 * 32 + t * 16 + g * 4 + j) * 576 + (nt0 + n) * 16 + c16]);
#pragma unroll
  for (int ks = 0; ks < 18; ++ks) {
    const bf16x8 a0 = *(const bf16x8*)(ofp + ks * 1024), a1 = *(const bf16x8*)(ofp + 18432 + ks * 1024);
#pragma unroll
    for (int n = 0; n < 3; ++n) {
      const bf16x8 bb = *(const bf16x8*)(wfp + n * 18432 + ks * 1024);
      acc[0][n] = MFMA16(a0, bb, acc[0][n]);
      acc[1][n] = MFMA16(a1, bb, acc[1][n]);
    }
  }
  float ps[2][4], pm[2][4];
#pragma unroll
  for (int t = 0; t < 2; ++t)
#pragma unroll
    for (int j = 0; j < 4; ++j) {
      float s = acc[t][0][j] + acc[t][1][j] + acc[t][2][j];
      s += __shfl_xor(s, 1); s += __shfl_xor(s, 2); s += __shfl_xor(s, 4); s += __shfl_xor(s, 8);
      const float mu = s * (1.f / 48.f);
      const float d0 = acc[t][0][j] - mu, d1 = acc[t][1][j] - mu, d2 = acc[t][2][j] - mu;
      float m2 = d0 * d0 + d1 * d1 + d2 * d2;
      m2 += __shfl_xor(m2, 1); m2 += __shfl_xor(m2, 2); m2 += __shfl_xor(m2, 4); m2 += __shfl_xor(m2, 8);
      ps[t][j] = s; pm[t][j] = m2;
    }
  float mine = 0.f;
#pragma unroll
  for (int t = 0; t < 2; ++t)
#pragma unroll
    for (int j = 0; j < 4; ++j) {
      if (c16 == t * 8 + j * 2) mine = ps[t][j];
      if (c16 == t * 8 + j * 2 + 1) mine = pm[t][j];
    }
  gu64* gr = (gu64*)(ws + WS_GRAN + (size_t)tt32 * 6144);
  __hip_atomic_store(gr + (ct * 3 + w) * 64 + lane, (1ull << 32) | (unsigned long long)__float_as_uint(mine), RLX_AGENT);
  float* hp = (float*)(ws + WS_H);
#pragma unroll
  for (int t = 0; t < 2; ++t)
#pragma unroll
    for (int n = 0; n < 3; ++n)
#pragma unroll
      for (int j = 0; j < 4; ++j)
        hp[(size_t)(tt32 * 32 + t * 16 + g * 4 + j) * 576 + (nt0 + n) * 16 + c16] = acc[t][n][j];
  float pv[12];
  for (unsigned spins = 0;; ++spins) {
    bool ok = true;
#pragma unroll
    for (int s = 0; s < 12; ++s) {
      const unsigned long long v = __hip_atomic_load(gr + s * 64 + lane, RLX_AGENT);
      pv[s] = __uint_as_float((unsigned)v);
      ok &= (v >> 32) == 1ull;
    }
    if (__all(ok) || spins > (1u << 22)) break;
    __builtin_amdgcn_s_sleep(1);
  }
  float tot = 0.f;
#pragma unroll
  for (int s = 0; s < 12; ++s) tot += pv[s];
  float extra = 0.f;
  const float mu_all = tot * (1.f / 576.f);
#pragma unroll
  for (int s = 0; s < 12; ++s) { const float d = pv[s] * (1.f / 48.f) - mu_all; extra += 48.f * d * d; }
  const float other = __shfl_xor(tot, 1);
  const float var = (other + extra) * (1.f / 576.f);
  const float r1 = rsqrtf(var + LN_EPS);
  const float r2 = rsqrtf(var * r1 * r1 + LN_EPS);
  const float rr = r1 * r2;
  char* hnp = ws + WS_HN;
#pragma unroll
  for (int t = 0; t < 2; ++t) {
    float mu[4], rs[4];
#pragma unroll
    for (int j = 0; j < 4; ++j) {
      const int src = (lane & 48) + t * 8 + j * 2;
      mu[j] = __shfl(mu_all, src); rs[j] = __shfl(rr, src);
    }
#pragma unroll
    for (int n = 0; n < 3; ++n) {
      u32x2 v;
      v[0] = cvtpk((acc[t][n][0] - mu[0]) * rs[0], (acc[t][n][1] - mu[1]) * rs[1]);
      v[1] = cvtpk((acc[t][n][2] - mu[2]) * rs[2], (acc[t][n][3] - mu[3]) * rs[3]);
      *(u32x2*)(hnp + ((size_t)((nt0 + n) * 64 + tt32) * 64 + (2 * t + (g >> 1)) * 16 + c16) * 16 + (g & 1) * 8) = v;
    }
  }
}

__device__ __forceinline__ float gelu_tanh(float v) {
  const float z = v * (0.7978845608028654f + 0.035677408136300125f * v * v);
  const float e = __builtin_amdgcn_exp2f(z * -2.8853900817779268f);
  return v * __builtin_amdgcn_rcpf(1.0f + e);
}
constexpr int G1_ST = 5, G1_SB = 28672;
__global__ __launch_bounds__(256, 1) void k4_gemm1(const u16* __restrict__ w1, const u16* __restrict__ b1, char* __restrict__ ws) {
  __shared__ __attribute__((aligned(16))) char lds[G1_ST * G1_SB];
  const int tid = threadIdx.x, lane = tid & 63;
  const int w = __builtin_amdgcn_readfirstlane(tid >> 6);
  const int id = (blockIdx.x & 7) * 27 + (blockIdx.x >> 3);
  const int pn = id / 6, pm = id - pn * 6;
  const int wr = w >> 1, wc = w & 1;
  const unsigned lds0 = (unsigned)(uintptr_t)lds;
  const int g = lane >> 4, c16 = lane & 15, q4 = (lane & 15) >> 2, p4 = lane & 3;
  const char* hn = ws + WS_HN + (size_t)(pm * 6) * 65536;
  unsigned aoff[3];
#pragma unroll
  for (int i = 0; i < 3; ++i) { const int a = w + 4 * i; aoff[i] = (unsigned)((a >> 1) * 65536 + (a & 1) * 1024 + lane * 16); }
  const char* wb = (const char*)w1 + (size_t)pn * 256;
  unsigned boff[4];
#pragma unroll
  for (int i = 0; i < 4; ++i) {
    const int row = (4 * w + i) * 4 + (lane >> 4), cp = lane & 15;
    const int f = (row & 3) | (((row >> 3) & 1) << 2);
    const int ch = (((cp >> 1) ^ f) << 1) | (cp & 1);
    boff[i] = (unsigned)(row * 9216 + ch * 16);
  }
#define G1_ISSUE(t, slot) do { const unsigned sl_ = lds0 + (unsigned)(slot) * G1_SB; \
    const char* ab_ = hn + (size_t)(t) * 2048; const char* bb_ = wb + (size_t)(t) * (64 * 9216); \
    _Pragma("unroll") for (int i = 0; i < 3; ++i) glds16(ab_, aoff[i], sl_ + (unsigned)(w + 4 * i) * 1024u); \
    _Pragma("unroll") for (int i = 0; i < 4; ++i) glds16(bb_, boff[i], sl_ + 12288u + (unsigned)(4 * w + i) * 1024u); } while (0)
#pragma unroll
  for (int t = 0; t < G1_ST - 1; ++t) G1_ISSUE(t, t);
  const int fl = q4 | ((g & 1) << 2);
  unsigned brd[4];
#pragma unroll
  for (int n = 0; n < 4; ++n) brd[n] = (unsigned)(12288 + (8 * g + q4) * 256 + (((wc * 4 + n) ^ fl) * 32) + 8 * p4);
  const unsigned ard = (unsigned)(wr * 6144 + lane * 16);
  f32x4 acc[4][3];
#pragma unroll
  for (int n = 0; n < 4; ++n)
#pragma unroll
    for (int m = 0; m < 3; ++m) acc[n][m] = (f32x4){0, 0, 0, 0};
  int slot = 0, islot = G1_ST - 1;
  for (int t = 0; t < 32; ++t) {
    if (t < 32 - (G1_ST - 2)) asm volatile("s_waitcnt vmcnt(21) lgkmcnt(0)\n\ts_barrier" ::: "memory");
    else asm volatile("s_waitcnt vmcnt(0) lgkmcnt(0)\n\ts_barrier" ::: "memory");
    if (t + G1_ST - 1 < 32) { G1_ISSUE(t + G1_ST - 1, islot); }
    islot = (islot + 1 == G1_ST) ? 0 : islot + 1;
    const lds_cptr sp = (lds_cptr)lds + slot * G1_SB;
#pragma unroll
    for (int ks = 0; ks < 2; ++ks) {
      bf16x8 af[3];
#pragma unroll
      for (int m = 0; m < 3; ++m) af[m] = ldsr128(sp + ard + m * 2048 + ks * 1024);
#pragma unroll
      for (int n = 0; n < 4; ++n) {
        const bf16x8 bf = cat8(trread(sp + brd[n] + ks * 8192), trread(sp + brd[n] + ks * 8192 + 1024));
#pragma unroll
        for (int m = 0; m < 3; ++m) acc[n][m] = MFMA16(bf, af[m], acc[n][m]);
      }
    }
    slot = (slot + 1 == G1_ST) ? 0 : slot + 1;
  }
  char* up = ws + WS_U;
#pragma unroll
  for (int n = 0; n < 4; ++n) {
    const int ft = pn * 8 + wc * 4 + n;
    const u32x2 bv = *(const u32x2*)(b1 + ft * 16 + g * 4);
    const float bb0 = __uint_as_float(bv[0] << 16), bb1 = __uint_as_float(bv[0] & 0xffff0000u);
    const float bb2 = __uint_as_float(bv[1] << 16), bb3 = __uint_as_float(bv[1] & 0xffff0000u);
#pragma unroll
    for (int m = 0; m < 3; ++m) {
      const int dt = pm * 6 + wr * 3 + m;
      u32x2 v;
      v[0] = cvtpk(gelu_tanh(acc[n][m][0] + bb0), gelu_tanh(acc[n][m][1] + bb1));
      v[1] = cvtpk(gelu_tanh(acc[n][m][2] + bb2), gelu_tanh(acc[n][m][3] + bb3));
      *(u32x2*)(up + ((size_t)(dt * 144 + (ft >> 1)) * 64 + ((ft & 1) * 2 + (g >> 1)) * 16 + c16) * 16 + (g & 1) * 8) = v;
    }
  }
#undef G1_ISSUE
}

constexpr int G2_ST = 4, G2_SB = 36864;
__global__ __launch_bounds__(256, 1) void k5_gemm2(const u16* __restrict__ w2, const u16* __restrict__ b2, char* __restrict__ ws,
                                                    u16* __restrict__ out) {
  __shared__ __attribute__((aligned(16))) char lds[G2_ST * G2_SB];
  const int tid = threadIdx.x, lane = tid & 63;
  const int w = __builtin_amdgcn_readfirstlane(tid >> 6);
  const int xg = blockIdx.x & 7, o = blockIdx.x >> 3;
  const int kq = o & 3, pm = (o >> 2) & 3, pn = xg * 2 + (o >> 4);
  const int tile = pn * 4 + pm;
  const unsigned lds0 = (unsigned)(uintptr_t)lds;
  const int g = lane >> 4, c16 = lane & 15, q4 = (lane & 15) >> 2, p4 = lane & 3;
  const char* ub = ws + WS_U + (size_t)(pm * 9) * 147456 + (size_t)kq * 36864;
  unsigned aoff[5];
#pragma unroll
  for (int i = 0; i < 5; ++i) { int a = w + 4 * i; if (a >= 18) a -= 4; aoff[i] = (unsigned)((a >> 1) * 147456 + (a & 1) * 1024 + lane * 16); }
  const char* wb = (const char*)w2 + (size_t)kq * 1152 * 4096 + (size_t)pn * 256;
  unsigned boff[4];
#pragma unroll
  for (int i = 0; i < 4; ++i) {
    const int row = (4 * w + i) * 4 + (lane >> 4), cp = lane & 15;
    const int f = (row & 3) | (((row >> 3) & 1) << 2);
    const int ch = (((cp >> 1) ^ f) << 1) | (cp & 1);
    boff[i] = (unsigned)(row * 4096 + ch * 16);
  }
#define G2_ISSUE(t, slot) do { const unsigned sl_ = lds0 + (unsigned)(slot) * G2_SB; \
    const char* ab_ = ub + (size_t)(t) * 2048; const char* bb_ = wb + (size_t)(t) * (64 * 4096); \
    _Pragma("unroll") for (int i = 0; i < 5; ++i) glds16(ab_, aoff[i], sl_ + (unsigned)(w + 4 * i) * 1024u); \
    _Pragma("unroll") for (int i = 0; i < 4; ++i) glds16(bb_, boff[i], sl_ + 20480u + (unsigned)(4 * w + i) * 1024u); } while (0)
#pragma unroll
  for (int t = 0; t < G2_ST - 1; ++t) G2_ISSUE(t, t);
  const int fl = q4 | ((g & 1) << 2);
  unsigned brd[2];
#pragma unroll
  for (int n = 0; n < 2; ++n) brd[n] = (unsigned)(20480 + (8 * g + q4) * 256 + (((w * 2 + n) ^ fl) * 32) + 8 * p4);
  const unsigned ard = (unsigned)(lane * 16);
  f32x4 acc[18];
#pragma unroll
  for (int i = 0; i < 18; ++i) acc[i] = (f32x4){0, 0, 0, 0};
  int slot = 0, islot = G2_ST - 1;
  for (int t = 0; t < 18; ++t) {
    if (t < 18 - (G2_ST - 2)) asm volatile("s_waitcnt vmcnt(18) lgkmcnt(0)\n\ts_barrier" ::: "memory");
    else asm volatile("s_waitcnt vmcnt(0) lgkmcnt(0)\n\ts_barrier" ::: "memory");
    if (t + G2_ST - 1 < 18) { G2_ISSUE(t + G2_ST - 1, islot); }
    islot = (islot + 1 == G2_ST) ? 0 : islot + 1;
    const lds_cptr sp = (lds_cptr)lds + slot * G2_SB;
#pragma unroll
    for (int ks = 0; ks < 2; ++ks) {
      bf16x8 bf[2];
#pragma unroll
      for (int n = 0; n < 2; ++n) bf[n] = cat8(trread(sp + brd[n] + ks * 8192), trread(sp + brd[n] + ks * 8192 + 1024));
#pragma unroll
      for (int m = 0; m < 9; ++m) {
        const bf16x8 af = ldsr128(sp + ard + m * 2048 + ks * 1024);
        acc[m * 2] = MFMA16(af, bf[0], acc[m * 2]);
        acc[m * 2 + 1] = MFMA16(af, bf[1], acc[m * 2 + 1]);
      }
    }
    slot = (slot + 1 == G2_ST) ? 0 : slot + 1;
  }
  const auto rsrc = __builtin_amdgcn_make_buffer_rsrc(ws + WS_SLAB + (size_t)tile * (16 * 20480), 0, 16 * 20480, 0x00020000);
  const int sbase = w * 5120 + lane * 16;
#pragma unroll
  for (int i = 0; i < 18; ++i) {
    const int dst = i & 3;
    if (dst != kq)
      __builtin_amdgcn_raw_buffer_store_b128(__builtin_bit_cast(u32x4, acc[i]), rsrc, (dst * 4 + kq) * 20480 + sbase + (i >> 2) * 1024, 0, 16);
  }
  asm volatile("s_waitcnt vmcnt(0)" ::: "memory");
  __syncthreads();
  gu32* cnt = (gu32*)(ws + WS_CNT + (size_t)tile * 256);
  if (tid == 0) {
    __hip_atomic_fetch_add(cnt, 1u, RLX_AGENT);
    unsigned spins = 0;
    while (__hip_atomic_load(cnt, RLX_AGENT) < 4u) { __builtin_amdgcn_s_sleep(1); if (++spins > (1u << 22)) break; }
  }
  __syncthreads();
  const float* hp = (const float*)(ws + WS_H);
#pragma unroll
  for (int i4 = 0; i4 < 5; ++i4) {
    const int tau = i4 * 4 + kq;
    if (tau < 18) {
      f32x4 a = (kq == 0) ? acc[i4 * 4] : (kq == 1) ? acc[i4 * 4 + 1] : (kq == 2) ? acc[(i4 * 4 + 2) % 18] : acc[(i4 * 4 + 3) % 18];
#pragma unroll
      for (int s = 1; s < 4; ++s) {
        const int src = (kq + s) & 3;
        const u32x4 v = __builtin_amdgcn_raw_buffer_load_b128(rsrc, (kq * 4 + src) * 20480 + sbase + i4 * 1024, 0, 16);
        a += __builtin_bit_cast(f32x4, v);
      }
      const int m = tau >> 1, n = tau & 1;
      const int sp_ = pn * 128 + (w * 2 + n) * 16 + c16;
      const int d0 = pm * 144 + m * 16 + g * 4;
      const f32x4 hv = *(const f32x4*)(hp + (size_t)sp_ * 576 + d0);
      const float bb = bf2f(b2[sp_]);
      u32x2 v;
      v[0] = cvtpk(a[0] + bb + hv[0], a[1] + bb + hv[1]);
      v[1] = cvtpk(a[2] + bb + hv[2], a[3] + bb + hv[3]);
      *(u32x2*)(out + (size_t)sp_ * 576 + d0) = v;
    }
  }
#undef G2_ISSUE
}

extern "C" void kernel_launch(void* const* d_in, const int* in_sizes, int n_in,
                              void* d_out, int out_size, void* d_ws, size_t ws_size,
                              hipStream_t stream) {
  const u16* x    = (const u16*)d_in[0];
  const u16* wqkv = (const u16*)d_in[1];
  const u16* wo   = (const u16*)d_in[2];
  const u16* w1   = (const u16*)d_in[4];
  const u16* b1   = (const u16*)d_in[5];
  const u16* w2   = (const u16*)d_in[6];
  const u16* b2   = (const u16*)d_in[7];
  char* ws = (char*)d_ws;
  k1_qkv<<<256, 256, 0, stream>>>(x, wqkv, ws);
  k2_attn<<<256, 1024, 0, stream>>>(ws, wo, w1, w2);
  k3_proj<<<256, 192, 0, stream>>>(x, ws);
  k4_gemm1<<<216, 256, 0, stream>>>(w1, b1, ws);
  k5_gemm2<<<256, 256, 0, stream>>>(w2, b2, ws, (u16*)d_out);
}
```

```cpp
#include <hip/hip_runtime.h>
#include <stdint.h>

typedef short    bf16x8 __attribute__((ext_vector_type(8)));
typedef short    s16x4  __attribute__((ext_vector_type(4)));
typedef float    f32x4  __attribute__((ext_vector_type(4)));
typedef float    f32x16 __attribute__((ext_vector_type(16)));
typedef unsigned u32x2  __attribute__((ext_vector_type(2)));
typedef unsigned u32x4  __attribute__((ext_vector_type(4)));
typedef __bf16   bf16x2_t __attribute__((ext_vector_type(2)));
typedef unsigned short u16;
#define LDS_AS __attribute__((address_space(3)))
#define GLB_AS __attribute__((address_space(1)))
typedef LDS_AS const char* lds_cptr;
typedef GLB_AS unsigned gu32;
typedef GLB_AS unsigned long long gu64;
#define RLX_AGENT __ATOMIC_RELAXED, __HIP_MEMORY_SCOPE_AGENT

constexpr int S_ = 2048, D_ = 576, F_ = 4608;
constexpr float LN_EPS = 1e-5f;
constexpr float QSCALE = 0.23570226039551584f * 1.4426950408889634f;

constexpr unsigned VREC    = 640;
constexpr unsigned KXOFF   = 65536 + 128 * VREC;
constexpr unsigned KVHEAD  = KXOFF + 8192;
constexpr size_t WS_QM   = 0;
constexpr size_t WS_QX   = WS_QM + 32u * 65536;
constexpr size_t WS_KV   = WS_QX + 32u * 8192;
constexpr size_t WS_OF   = WS_KV + 32u * KVHEAD;
constexpr size_t WS_WOF  = WS_OF + 128u * 18 * 1024;
constexpr size_t WS_HN   = WS_WOF + 36u * 18 * 1024;
constexpr size_t WS_H    = WS_HN + 36u * 64 * 1024;
constexpr size_t WS_U    = WS_H + 2048u * 576 * 4;
constexpr size_t WS_GRAN = WS_U + 36u * 144 * 1024;
constexpr size_t WS_SLAB = WS_GRAN + 64u * 4 * 512;
constexpr unsigned SLABT = 16u * 10240;

__device__ __forceinline__ unsigned cvtpk(float lo, float hi) {
  typedef float f32x2_t __attribute__((ext_vector_type(2)));
  return __builtin_bit_cast(unsigned, __builtin_convertvector((f32x2_t){lo, hi}, bf16x2_t));
}
__device__ __forceinline__ float bf2f(u16 v) { return __uint_as_float((unsigned)v << 16); }
__device__ __forceinline__ void glds16(const void* sbase, unsigned voff, unsigned lds_base) {
  unsigned sv;
  asm volatile("s_mov_b32 %0, m0\n\ts_mov_b32 m0, %3\n\ts_nop 0\n\tglobal_load_lds_dwordx4 %1, %2\n\ts_mov_b32 m0, %0"
               : "=&s"(sv) : "v"(voff), "s"(sbase), "s"(lds_base) : "memory");
}
__device__ __forceinline__ s16x4 trread(lds_cptr p) {
  typedef short v4i16_t __attribute__((ext_vector_type(4)));
  return __builtin_bit_cast(s16x4, __builtin_amdgcn_ds_read_tr16_b64_v4i16((LDS_AS v4i16_t*)p));
}
__device__ __forceinline__ bf16x8 cat8(s16x4 a, s16x4 b) {
  return (bf16x8){a[0], a[1], a[2], a[3], b[0], b[1], b[2], b[3]};
}
__device__ __forceinline__ bf16x8 ldsr128(lds_cptr p) { return *(const LDS_AS bf16x8*)p; }
#define MFMA16(a, b, c) __builtin_amdgcn_mfma_f32_16x16x32_bf16((a), (b), (c), 0, 0, 0)
#define MFMA32(a, b, c) __builtin_amdgcn_mfma_f32_32x32x16_bf16((a), (b), (c), 0, 0, 0)
#define SB0() __builtin_amdgcn_sched_barrier(0)
#define SGB(mask, n) __builtin_amdgcn_sched_group_barrier((mask), (n), 0)
#define WAITV(n) asm volatile("s_waitcnt vmcnt(" #n ")" ::: "memory")
#define BARRIER() asm volatile("s_waitcnt lgkmcnt(0)\n\ts_barrier" ::: "memory")
__device__ __forceinline__ float dot2(unsigned a, unsigned b, float c) {
  return __builtin_amdgcn_fdot2_f32_bf16(__builtin_bit_cast(bf16x2_t, a), __builtin_bit_cast(bf16x2_t, b), c, false);
}
constexpr unsigned ONES2 = 0x3F803F80u;
#define STAMP(kid, i) do {} while (0)
#define TSTAMP(i) do {} while (0)
#define TFLUSH(kid, a, b) do {} while (0)

constexpr int Q_ST = 5, Q_SB = 30720;
__global__ __launch_bounds__(512, 1) void k1_qkv(const u16* __restrict__ x, const u16* __restrict__ wqkv, char* __restrict__ ws) {
  __shared__ __attribute__((aligned(16))) char lds[Q_ST * Q_SB + 512];
  const int tid = threadIdx.x, lane = tid & 63;
  const int w8 = __builtin_amdgcn_readfirstlane(tid >> 6), w = w8 & 3;
  const int b = blockIdx.x, xg = b & 7, o = b >> 3;
  const int bm = (xg >> 1) * 4 + (o & 3), hg = (xg & 1) * 8 + (o >> 2);
  const unsigned lds0 = (unsigned)(uintptr_t)lds;
  float* csl = (float*)(lds + Q_ST * Q_SB);
  constexpr int SP = 304;
  STAMP(0, 0);
  if (w8 >= 4) {
    const char* xb = (const char*)x + (size_t)bm * 128 * 1152;
    const char* wb = (const char*)wqkv + (216 * hg - 8 * (hg & 1));
    unsigned xoff[4], woff[4];
#pragma unroll
    for (int i = 0; i < 4; ++i) {
      const int row = (w + 4 * i) * 8 + (lane >> 3);
      xoff[i] = (unsigned)(row * 1152 + (((lane & 7) ^ (lane >> 3)) * 16));
      int pc = w + 4 * i; if (pc >= 14) pc -= 4;
      const unsigned c = (unsigned)(pc * 64 + lane), pos = c / 14u, ch = c - pos * 14u;
      const unsigned k = (pos & ~12u) | ((pos & 4u) << 1) | ((pos & 8u) >> 1);
      woff[i] = k * 3456u + ch * 16u;
    }
#define Q_ISSUE(NW, t, slot) do { const unsigned sl_ = lds0 + (unsigned)(slot) * Q_SB; \
    const char* xb_ = xb + (size_t)(t) * 128; const char* wb_ = wb + (size_t)(t) * (64 * 3456); \
    _Pragma("unroll") for (int i = 0; i < NW; ++i) glds16(wb_, woff[i], sl_ + 16384u + (unsigned)(w + 4 * i) * 1024u); \
    _Pragma("unroll") for (int i = 0; i < 4; ++i) glds16(xb_, xoff[i], sl_ + (unsigned)(w + 4 * i) * 1024u); } while (0)
#define Q_LOADER(NW, W3_, W2_, W1_) do { \
    Q_ISSUE(NW, 0, 0); Q_ISSUE(NW, 1, 1); Q_ISSUE(NW, 2, 2); Q_ISSUE(NW, 3, 3); \
    asm volatile("s_waitcnt vmcnt(" #W3_ ")\n\ts_barrier" ::: "memory"); Q_ISSUE(NW, 4, 4); \
    asm volatile("s_waitcnt vmcnt(" #W3_ ")\n\ts_barrier" ::: "memory"); Q_ISSUE(NW, 5, 0); \
    asm volatile("s_waitcnt vmcnt(" #W3_ ")\n\ts_barrier" ::: "memory"); Q_ISSUE(NW, 6, 1); \
    asm volatile("s_waitcnt vmcnt(" #W3_ ")\n\ts_barrier" ::: "memory"); Q_ISSUE(NW, 7, 2); \
    asm volatile("s_waitcnt vmcnt(" #W3_ ")\n\ts_barrier" ::: "memory"); Q_ISSUE(NW, 8, 3); \
    asm volatile("s_waitcnt vmcnt(" #W3_ ")\n\ts_barrier" ::: "memory"); \
    asm volatile("s_waitcnt vmcnt(" #W2_ ")\n\ts_barrier" ::: "memory"); \
    asm volatile("s_waitcnt vmcnt(" #W1_ ")\n\ts_barrier" ::: "memory"); \
    asm volatile("s_waitcnt vmcnt(0)\n\ts_barrier" ::: "memory"); } while (0)
    if (w < 2) Q_LOADER(4, 24, 16, 8); else Q_LOADER(3, 21, 14, 7);
    if (tid - 256 < 32) *(u32x4*)(ws + WS_GRAN + (size_t)b * 512 + (tid - 256) * 16) = (u32x4){0, 0, 0, 0};
    BARRIER();
    BARRIER();
  } else {
    const int g = lane >> 4, c16 = lane & 15, q4 = (lane & 15) >> 2, p4 = lane & 3;
    unsigned wrd[7];
#pragma unroll
    for (int i = 0; i < 7; ++i) {
      int n = i + 2 * w; if (n >= 7) n -= 7;
      int Q = 4 * n + p4; if (Q > 26) Q = 26;
      wrd[i] = (unsigned)(16384 + ((g >> 1) * 16 + (g & 1) * 4 + q4) * 224 + 8 * Q + 8 * (hg & 1));
    }
    const unsigned xrd0 = (unsigned)((w * 32 + c16) * 128 + ((g ^ (c16 & 7)) * 16)), xrd1 = xrd0 ^ 64u;
    f32x4 acc[7][2], csa[2], sx[2], sq[2];
#pragma unroll
    for (int n = 0; n < 7; ++n) { acc[n][0] = (f32x4){0, 0, 0, 0}; acc[n][1] = (f32x4){0, 0, 0, 0}; }
#pragma unroll
    for (int i = 0; i < 2; ++i) { csa[i] = (f32x4){0, 0, 0, 0}; sx[i] = (f32x4){0, 0, 0, 0}; sq[i] = (f32x4){0, 0, 0, 0}; }
    const bf16x8 ones8 = __builtin_bit_cast(bf16x8, (u32x4){ONES2, ONES2, ONES2, ONES2});
    bf16x8 wX[7], xX[2], wY[7], xY[2];
    int slot = 0;
#define Q_READ(W_, X_, ks) do { const lds_cptr sp_ = (lds_cptr)lds + slot * Q_SB; \
    X_[0] = ldsr128(sp_ + ((ks) ? xrd1 : xrd0)); X_[1] = ldsr128(sp_ + ((ks) ? xrd1 : xrd0) + 2048); \
    _Pragma("unroll") for (int n = 0; n < 7; ++n) W_[n] = cat8(trread(sp_ + wrd[n] + (ks) * 7168), trread(sp_ + wrd[n] + (ks) * 7168 + 1792)); } while (0)
#define Q_MMA(W_, X_) do { \
    sx[0] = MFMA16(ones8, X_[0], sx[0]); sx[1] = MFMA16(ones8, X_[1], sx[1]); \
    sq[0] = MFMA16(X_[0], X_[0], sq[0]); sq[1] = MFMA16(X_[1], X_[1], sq[1]); \
    _Pragma("unroll") for (int n = 0; n < 7; ++n) { acc[n][0] = MFMA16(W_[n], X_[0], acc[n][0]); acc[n][1] = MFMA16(W_[n], X_[1], acc[n][1]); \
      if (n < 2) csa[n] = MFMA16(W_[n], ones8, csa[n]); } } while (0)
#define Q_ILV() do { _Pragma("unroll") for (int i_ = 0; i_ < 8; ++i_) { SGB(0x8, 1); SGB(0x100, 2); } SGB(0x8, 12); } while (0)
    asm volatile("" :: "s"(ws));
    BARRIER();
    STAMP(0, 1);
    __builtin_amdgcn_s_waitcnt(0xC07F);
    Q_READ(wX, xX, 0);
    SB0();
    for (int t = 0; t < 8; ++t) {
      Q_READ(wY, xY, 1); Q_MMA(wX, xX); Q_ILV(); SB0();
      BARRIER();
      slot = (slot + 1 == Q_ST) ? 0 : slot + 1;
      SB0();
      Q_READ(wX, xX, 0); Q_MMA(wY, xY); Q_ILV(); SB0();
    }
    Q_READ(wY, xY, 1); Q_MMA(wX, xX); Q_ILV(); SB0();
    Q_MMA(wY, xY);
    STAMP(0, 2);
    if (c16 == 0) { *(f32x4*)(csl + (2 * w) * 16 + g * 4) = csa[0]; if (w < 3) *(f32x4*)(csl + (2 * w + 1) * 16 + g * 4) = csa[1]; }
    float mean[2], rstd[2];
#pragma unroll
    for (int tt = 0; tt < 2; ++tt) {
      const int j = c16 & 3;
      const float dg = (j == 0) ? sq[tt][0] : (j == 1) ? sq[tt][1] : (j == 2) ? sq[tt][2] : sq[tt][3];
      const float ss = __shfl(dg, (c16 >> 2) * 16 + c16);
      mean[tt] = sx[tt][0] * (1.f / 576.f);
      rstd[tt] = rsqrtf(fmaxf(ss * (1.f / 576.f) - mean[tt] * mean[tt], 0.f) + LN_EPS);
    }
    BARRIER();
#pragma unroll
    for (int n = 0; n < 7; ++n) {
      int nt = n + 2 * w; if (nt >= 7) nt -= 7;
      const f32x4 cs4 = *(const f32x4*)(csl + nt * 16 + g * 4);
#pragma unroll
      for (int e = 0; e < 2; ++e) {
        const unsigned col = (unsigned)(nt * 16 + g * 4 + e * 2);
        const unsigned hl = col / 18u;
        const unsigned so = (unsigned)((w * 32 + c16) * SP) + col * 2u + hl * 12u;
        const float qs = ((unsigned)hg * 6u + hl < 32u) ? QSCALE : 1.0f;
#pragma unroll
        for (int tt = 0; tt < 2; ++tt) {
          const float rs = rstd[tt] * qs;
          const float v0 = (acc[n][tt][2 * e] - mean[tt] * cs4[2 * e]) * rs;
          const float v1 = (acc[n][tt][2 * e + 1] - mean[tt] * cs4[2 * e + 1]) * rs;
          *(unsigned*)(lds + so + tt * 16 * SP) = cvtpk(v0, v1);
        }
      }
    }
    BARRIER();
  }
  STAMP(0, 3);
  {
    const unsigned t8 = (unsigned)tid & 255u;
    const unsigned Tl = t8 >> 1, c = t8 & 1u, T = (unsigned)bm * 128u + Tl;
    const unsigned oQK = (T >> 5) * 1024u + c * 512u + (T & 31u) * 16u;
    const unsigned T2 = (unsigned)bm * 128u + (t8 & 127u);
    const unsigned oKX = KXOFF + (T2 >> 7) * 512u + (T2 & 31u) * 16u + ((T2 >> 5) & 3u) * 4u;
    const unsigned vrec = t8 >> 5, vhh = (t8 >> 4) & 1u, vi = t8 & 15u;
    const lds_cptr vsrc = (lds_cptr)lds + ((vrec * 16u + vhh * 4u + (vi >> 2)) * SP + (vi & 3u) * 8u);
    const lds_cptr vsrx = (lds_cptr)lds + ((vrec * 16u + vhh * 4u + (vi >> 2)) * SP + 32u);
    const unsigned oV = 65536u + ((unsigned)bm * 8u + vrec) * VREC + vhh * 320u + vi * 16u;
#pragma unroll
    for (int i = 0; i < 3; ++i) {
      const int hl = (w8 >> 2) * 3 + i;
      const int hs = hg * 6 + hl, which = hs >> 5, h = hs & 31;
      char* base = ws + (which == 0 ? WS_QM + (size_t)h * 65536 : WS_KV + (size_t)h * KVHEAD);
      if (which == 2) {
        const bf16x8 vm = cat8(trread(vsrc + hl * 48), trread(vsrc + hl * 48 + 8 * SP));
        bf16x8 vx = cat8(trread(vsrx + hl * 48), trread(vsrx + hl * 48 + 8 * SP));
        *(bf16x8*)(base + oV) = vm;
        if (vi == 2u) vx = __builtin_bit_cast(bf16x8, (u32x4){ONES2, ONES2, ONES2, ONES2});
        if (vi == 3u) vx = (bf16x8){0, 0, 0, 0, 0, 0, 0, 0};
        if (vi < 4u) *(bf16x8*)(base + oV + 256u) = vx;
      } else {
        *(u32x4*)(base + oQK) = *(const u32x4*)(lds + Tl * SP + hl * 48 + c * 16);
        if (t8 < 128u) {
          const unsigned xv = *(const unsigned*)(lds + t8 * SP + hl * 48 + 32);
          if (which == 0) *(unsigned*)(ws + WS_QX + (size_t)h * 8192 + T2 * 4u) = xv;
          else *(unsigned*)(base + oKX) = xv;
        }
      }
    }
  }
  STAMP(0, 4);
#undef Q_ISSUE
#undef Q_LOADER
#undef Q_READ
#undef Q_MMA
#undef Q_ILV
}

#define PF_W1 9
#define PF_W2 9
#define EX2(v) __builtin_amdgcn_exp2f(v)
__global__ __launch_bounds__(512, 1) void k2_attn(char* __restrict__ ws, const u16* __restrict__ wo,
                                                   const u16* __restrict__ w1, const u16* __restrict__ w2) {
  __shared__ __attribute__((aligned(16))) char lds[KVHEAD + 1024];
  const int tid = threadIdx.x, lane = tid & 63;
  const int w = __builtin_amdgcn_readfirstlane(tid >> 6);
  const int b = blockIdx.x, xg = b & 7;
  const int h = xg * 4 + ((b >> 3) & 3), qt = b >> 5;
  const int qp = w & 3, half = w >> 2;
  const unsigned lds0 = (unsigned)(uintptr_t)lds;
  const int r = lane & 31, hh = lane >> 5;
  const char* kvg = ws + WS_KV + (size_t)h * KVHEAD;
  const unsigned l16 = (unsigned)lane * 16u;
  STAMP(1, 0);
  u32x4 qmA, qmB; unsigned qxA, qxB;
  {
    const char* qmp = ws + WS_QM + (size_t)h * 65536 + (size_t)(qt * 8 + qp * 2) * 1024 + lane * 16;
    const char* qxp = ws + WS_QX + (size_t)h * 8192 + (size_t)(qt * 256 + qp * 64 + r) * 4;
    asm volatile("global_load_dwordx4 %0, %1, off" : "=v"(qmA) : "v"(qmp) : "memory");
    asm volatile("global_load_dwordx4 %0, %1, off offset:1024" : "=v"(qmB) : "v"(qmp) : "memory");
    asm volatile("global_load_dword %0, %1, off" : "=v"(qxA) : "v"(qxp) : "memory");
    asm volatile("global_load_dword %0, %1, off offset:128" : "=v"(qxB) : "v"(qxp) : "memory");
  }
#pragma unroll
  for (int i = 0; i < 6; ++i) {
    const int s = i * 8 + w;
    const int p = (s < 10) ? s : (s < 20) ? 32 + s - 10 : (s < 30) ? 64 + s - 20 : (s < 40) ? 104 + s - 30 : 144 + s - 40;
    glds16(kvg + (size_t)p * 1024, l16, lds0 + p * 1024);
  }
#define STAGEB(i) do { const int s_ = (i) * 8 + w; \
    const int p_ = (s_ < 22) ? 10 + s_ : (s_ < 44) ? 42 + s_ - 22 : (s_ < 74) ? 74 + s_ - 44 : 114 + s_ - 74; \
    glds16(kvg + (size_t)p_ * 1024, l16, lds0 + p_ * 1024); } while (0)
  const lds_cptr vp = (lds_cptr)lds + (65536u + (unsigned)half * 64u * VREC + hh * 320 + (r < 19 ? r : 19) * 16);
  const lds_cptr kp = (lds_cptr)lds + (half * 32768 + hh * 512 + r * 16);
  const lds_cptr xp = (lds_cptr)lds + (KXOFF + half * 4096 + r * 16);

  u16 wog[8];
  const int wtask = b * 162 + (tid - 256);
  const bool wact = (tid >= 256) && (tid < 256 + 162);
  {
    const int tk = wact ? wtask : b * 162;
    const int l_ = tk & 63, ks = (tk >> 6) % 18, nt = tk / (64 * 18);
    const int g_ = l_ >> 4, n_ = nt * 16 + (l_ & 15);
    const int k0 = (ks < 16) ? 18 * (2 * ks + (g_ >> 1)) + 4 * (g_ & 1) : 18 * (16 * (ks - 16) + 4 * g_) + 16;
    const u16* wp_ = wo + k0 * 576 + n_;
#pragma unroll
    for (int j = 0; j < 8; ++j) {
      const int dk = (ks < 16) ? (j & 3) + 8 * (j >> 2) : 18 * (j >> 1) + (j & 1);
      wog[j] = wp_[dk * 576];
    }
  }
  const int gw = (b >> 3) * 8 + w;
  unsigned pfo = (unsigned)(lane >> 5) * 4096u + (lane & 31) * 16;
  const char* pfb = (const char*)w2 + (size_t)(gw * 2) * 4096 + xg * 512;
  size_t pfs = (size_t)512 * 4096;
  asm volatile("s_waitcnt vmcnt(8)\n\ts_barrier" : "+v"(qmA), "+v"(qmB), "+v"(qxA), "+v"(qxB) :: "memory");
  STAMP(1, 1);
  const bf16x8 qA = __builtin_bit_cast(bf16x8, qmA), qB = __builtin_bit_cast(bf16x8, qmB);
  bf16x8 eA[4], eB[4];
  {
    const unsigned a = hh == 0 ? qxA : 0u, bq = hh == 0 ? qxB : 0u;
    eA[0] = __builtin_bit_cast(bf16x8, (u32x4){a, 0u, 0u, 0u}); eB[0] = __builtin_bit_cast(bf16x8, (u32x4){bq, 0u, 0u, 0u});
    eA[1] = __builtin_bit_cast(bf16x8, (u32x4){0u, a, 0u, 0u}); eB[1] = __builtin_bit_cast(bf16x8, (u32x4){0u, bq, 0u, 0u});
    eA[2] = __builtin_bit_cast(bf16x8, (u32x4){0u, 0u, a, 0u}); eB[2] = __builtin_bit_cast(bf16x8, (u32x4){0u, 0u, bq, 0u});
    eA[3] = __builtin_bit_cast(bf16x8, (u32x4){0u, 0u, 0u, a}); eB[3] = __builtin_bit_cast(bf16x8, (u32x4){0u, 0u, 0u, bq});
  }
  const f32x16 zero16 = {};
  f32x16 oA = {}, oB = {}, sA, sB;
  u32x4 pAl = {0u, 0u, 0u, 0u}, pAh = {0u, 0u, 0u, 0u}, pBl = {0u, 0u, 0u, 0u}, pBh = {0u, 0u, 0u, 0u};
  bf16x8 kf, kx, kxn, v0, v1 = {};
  kf = ldsr128(kp); kx = ldsr128(xp);
  sA = MFMA32(kf, qA, zero16); sA = MFMA32(kx, eA[0], sA);
  float e0 = 0.f, e1 = 0.f, e2 = 0.f, e3 = 0.f, f0, f1, f2, f3;
  SB0();
#define EX4(a, b_, c_, d_, S, i0) do { a = EX2(S[i0]); b_ = EX2(S[i0 + 1]); c_ = EX2(S[i0 + 2]); d_ = EX2(S[i0 + 3]); } while (0)
#define CV2(P, j0, a, b_, c_, d_) do { P[j0] = cvtpk(a, b_); P[j0 + 1] = cvtpk(c_, d_); } while (0)
#define PF8(x) __builtin_bit_cast(bf16x8, x)
#define PIN(x) asm volatile("" : "+v"(x))
#define PINE() asm volatile("" : "+v"(e0), "+v"(e1), "+v"(e2), "+v"(e3))
#define PINF() asm volatile("" : "+v"(f0), "+v"(f1), "+v"(f2), "+v"(f3))
#define EXA(a, b_, S, i0) do { a = EX2(S[i0]); b_ = EX2(S[i0 + 1]); asm volatile("" : "+v"(a), "+v"(b_)); SB0(); } while (0)
#define EXB(c_, d_, S, i0) do { c_ = EX2(S[i0 + 2]); d_ = EX2(S[i0 + 3]); } while (0)
#define TILE(t) do { \
    constexpr int ro_ = (t) * 2 * (int)VREC; \
      sB = MFMA32(kf, qB, zero16); PIN(sB); SB0(); v0 = ldsr128(vq + ro_); kf = ldsr128(kq + ((t) + 1) * 1024); \
           if ((t) == 3) { kxn = ldsr128(xq + 512); } \
           EX4(f0, f1, f2, f3, sA, 0); CV2(pBh, 2, e0, e1, e2, e3); PINF(); PIN(pBh); SB0(); \
      EXA(e0, e1, sA, 4); oB = MFMA32(v1, PF8(pBh), oB); PIN(oB); SB0(); v1 = ldsr128(vq + ro_ + VREC); \
           EXB(e2, e3, sA, 4); CV2(pAl, 0, f0, f1, f2, f3); PINE(); PIN(pAl); SB0(); \
      EXA(f0, f1, sA, 8); sB = MFMA32(kx, eB[t], sB); PIN(sB); SB0(); \
           EXB(f2, f3, sA, 8); CV2(pAl, 2, e0, e1, e2, e3); PINF(); PIN(pAl); SB0(); \
      EXA(e0, e1, sA, 12); __builtin_amdgcn_s_waitcnt(0xC07F); SB0(); oA = MFMA32(v0, PF8(pAl), oA); PIN(oA); SB0(); \
           EXB(e2, e3, sA, 12); CV2(pAh, 0, f0, f1, f2, f3); PINE(); PIN(pAh); SB0(); \
      EXA(f0, f1, sB, 0); sA = MFMA32(kf, qA, zero16); PIN(sA); SB0(); EXB(f2, f3, sB, 0); CV2(pAh, 2, e0, e1, e2, e3); PINF(); PIN(pAh); SB0(); \
      EXA(e0, e1, sB, 4); oA = MFMA32(v1, PF8(pAh), oA); PIN(oA); SB0(); EXB(e2, e3, sB, 4); CV2(pBl, 0, f0, f1, f2, f3); PINE(); PIN(pBl); SB0(); \
      EXA(f0, f1, sB, 8); sA = MFMA32((t) == 3 ? kxn : kx, eA[((t) + 1) & 3], sA); PIN(sA); SB0(); EXB(f2, f3, sB, 8); CV2(pBl, 2, e0, e1, e2, e3); PINF(); PIN(pBl); SB0(); \
      EXA(e0, e1, sB, 12); oB = MFMA32(v0, PF8(pBl), oB); PIN(oB); SB0(); EXB(e2, e3, sB, 12); CV2(pBh, 0, f0, f1, f2, f3); PINE(); PIN(pBh); SB0(); \
  } while (0)
#define SIDE(a) do { STAGEB(it * 8 + (a)); if (it * 8 + (a) + 1 < 13) STAGEB(it * 8 + (a) + 1); SB0(); } while (0)
#define PFETCH() do { glds16(pfb, pfo, lds0 + KVHEAD); pfb += pfs; SB0(); } while (0)
  lds_cptr vq = vp, kq = kp, xq = xp;
  const auto slabr = __builtin_amdgcn_make_buffer_rsrc(ws + WS_SLAB + (size_t)b * 40960, 0, 40960, 0x00020000);
  __builtin_amdgcn_s_setprio(3);
#pragma unroll 1
  for (int it = 0; it < 2; ++it) {
    SIDE(0); TILE(0);
    SIDE(2); TILE(1);
    SIDE(4); TILE(2);
    if (it == 0) SIDE(6);
    TILE(3);
    kx = kxn;
    vq += 8 * VREC; kq += 4096; xq += 512;
  }
  asm volatile("s_waitcnt vmcnt(0)\n\ts_barrier" ::: "memory");
  STAMP(1, 2);
#pragma unroll 1
  for (int it = 2; it < 8; ++it) {
    if (it == 5) {
      pfo = (unsigned)(lane >> 3) * 9216u + (lane & 7) * 16;
      pfb = (const char*)w1 + (size_t)(gw * 8) * 9216 + xg * 1152; pfs = 128;
    }
    if (it == 4) __builtin_amdgcn_s_setprio(2);
    if (it == 6) __builtin_amdgcn_s_setprio(1);
    if (it == 7) __builtin_amdgcn_s_setprio(0);
    SB0();
    PFETCH(); TILE(0);
    PFETCH(); TILE(1);
    PFETCH(); TILE(2);
    if (it < 7) {
      __builtin_amdgcn_raw_buffer_store_b128((u32x4){~0u, ~0u, ~0u, ~0u}, slabr, (w * 5 + it - 2) * 1024 + lane * 16, 0, 16);
      SB0();
    }
    TILE(3);
    kx = kxn;
    vq += 8 * VREC; kq += 4096; xq += 512;
  }
  CV2(pBh, 2, e0, e1, e2, e3);
  oB = MFMA32(v1, PF8(pBh), oB);
  STAMP(1, 3);

  float mA = 0.f, mB = 0.f;
  {
    bool bad = false;
    if (hh == 0) {
      bad = !(oA[10] >= 8.271806125530277e-25f && oA[10] <= 1.2676506002282294e30f) ||
            !(oB[10] >= 8.271806125530277e-25f && oB[10] <= 1.2676506002282294e30f);
    }
#pragma unroll
    for (int i = 0; i < 10; ++i) bad |= !(__builtin_fabsf(oA[i]) <= 3.0e38f) || !(__builtin_fabsf(oB[i]) <= 3.0e38f);
    if (__any(bad)) {
#pragma unroll
      for (int qq = 0; qq < 2; ++qq) {
        const bf16x8 qf = qq ? qB : qA;
        f32x16 oacc = zero16;
        float m = -INFINITY;
        for (int it = 0; it < 8; ++it) {
          const bf16x8 kx_ = ldsr128(xp + it * 512);
#pragma unroll
          for (int t = 0; t < 4; ++t) {
            const bf16x8 kf_ = ldsr128(kp + (it * 4 + t) * 1024);
            f32x16 s_ = MFMA32(kf_, qf, zero16);
            s_ = MFMA32(kx_, qq ? eB[t] : eA[t], s_);
            float tm = s_[0];
#pragma unroll
            for (int i = 1; i < 16; ++i) tm = fmaxf(tm, s_[i]);
            tm = fmaxf(tm, __shfl_xor(tm, 32));
            const float mn = fmaxf(m, tm);
            const float sc = EX2(m - mn);
            m = mn;
#pragma unroll
            for (int i = 0; i < 16; ++i) oacc[i] *= sc;
            const int ro_ = (it * 8 + t * 2) * (int)VREC;
            const bf16x8 v0_ = ldsr128(vp + ro_), v1_ = ldsr128(vp + ro_ + VREC);
            u32x4 pa_, pb_;
#pragma unroll
            for (int i = 0; i < 4; ++i) {
              pa_[i] = cvtpk(EX2(s_[2 * i] - m), EX2(s_[2 * i + 1] - m));
              pb_[i] = cvtpk(EX2(s_[8 + 2 * i] - m), EX2(s_[8 + 2 * i + 1] - m));
            }
            oacc = MFMA32(v0_, PF8(pa_), oacc);
            oacc = MFMA32(v1_, PF8(pb_), oacc);
          }
        }
        if (qq) { oB = oacc; mB = m; } else { oA = oacc; mA = m; }
      }
    }
  }
  __syncthreads();
  {
    f32x4* mb = (f32x4*)(lds + w * 4096);
    float os[11];
#pragma unroll
    for (int i = 0; i < 11; ++i) os[i] = half ? oA[i] : oB[i];
    const float ms = half ? mA : mB;
    mb[lane] = (f32x4){os[0], os[1], os[2], os[3]};
    mb[64 + lane] = (f32x4){os[4], os[5], os[6], os[7]};
    mb[128 + lane] = (f32x4){os[8], os[9], os[10], ms};
  }
  __syncthreads();
  {
    const f32x4* mb = (const f32x4*)(lds + (w ^ 4) * 4096);
    float ok[11];
#pragma unroll
    for (int i = 0; i < 11; ++i) ok[i] = half ? oB[i] : oA[i];
    const float mk = half ? mB : mA;
    const f32x4 a0 = mb[lane], a1 = mb[64 + lane], a2 = mb[128 + lane];
    const float M = fmaxf(mk, a2[3]);
    const float s0 = EX2(mk - M), s1 = EX2(a2[3] - M);
    const float lsum = __shfl(ok[10] * s0 + a2[2] * s1, r);
    const float inv = 1.0f / lsum;
    const float c0 = s0 * inv, c1 = s1 * inv;
    u32x4 ov;
    ov[0] = cvtpk(ok[0] * c0 + a0[0] * c1, ok[1] * c0 + a0[1] * c1);
    ov[1] = cvtpk(ok[2] * c0 + a0[2] * c1, ok[3] * c0 + a0[3] * c1);
    ov[2] = cvtpk(ok[4] * c0 + a1[0] * c1, ok[5] * c0 + a1[1] * c1);
    ov[3] = cvtpk(ok[6] * c0 + a1[2] * c1, ok[7] * c0 + a1[3] * c1);
    const int T = qt * 256 + qp * 64 + half * 32 + r;
    char* ofb = ws + WS_OF + (size_t)(T >> 4) * 18432;
    *(u32x4*)(ofb + (h >> 1) * 1024 + (((h & 1) * 2 + hh) * 16 + (T & 15)) * 16) = ov;
    if (hh == 0)
      *(unsigned*)(ofb + (16 + (h >> 4)) * 1024 + (((h & 15) >> 2) * 16 + (T & 15)) * 16 + (h & 3) * 4) =
          cvtpk(ok[8] * c0 + a2[0] * c1, ok[9] * c0 + a2[1] * c1);
  }
  if (wact) {
    u32x4 v;
#pragma unroll
    for (int j = 0; j < 4; ++j) v[j] = (unsigned)wog[2 * j] | ((unsigned)wog[2 * j + 1] << 16);
    *(u32x4*)(ws + WS_WOF + (size_t)wtask * 16) = v;
  }
  STAMP(1, 4);
  asm volatile("s_waitcnt vmcnt(0)" ::: "memory");
#undef TILE
#undef SIDE
#undef PFETCH
#undef STAGEB
#undef EX4
#undef CV2
}

__device__ __forceinline__ float row16_sum(float v) {
  v += __builtin_bit_cast(float, __builtin_amdgcn_update_dpp(0, __builtin_bit_cast(int, v), 0x128, 0xf, 0xf, false));
  v += __builtin_bit_cast(float, __builtin_amdgcn_update_dpp(0, __builtin_bit_cast(int, v), 0x124, 0xf, 0xf, false));
  v += __builtin_bit_cast(float, __builtin_amdgcn_update_dpp(0, __builtin_bit_cast(int, v), 0x122, 0xf, 0xf, false));
  v += __builtin_bit_cast(float, __builtin_amdgcn_update_dpp(0, __builtin_bit_cast(int, v), 0x121, 0xf, 0xf, false));
  return v;
}
__global__ __launch_bounds__(576, 1) void k3_proj(const u16* __restrict__ x, char* __restrict__ ws) {
  __shared__ __attribute__((aligned(16))) char lds[36864 + 9 * 256 + 256];
  const int tid = threadIdx.x, lane = tid & 63;
  const int w = __builtin_amdgcn_readfirstlane(tid >> 6);
  const int b = blockIdx.x, xg = b & 7, o = b >> 3;
  const int tt32 = xg * 8 + (o >> 2), ct = o & 3;
  const int g = lane >> 4, c16 = lane & 15;
  const int nt = ct * 9 + w;
  const unsigned lds0 = (unsigned)(uintptr_t)lds;
  STAMP(2, 0);
#pragma unroll
  for (int i = 0; i < 4; ++i)
    glds16(ws + WS_OF + (size_t)(tt32 * 2) * 18432 + (size_t)(i * 9 + w) * 1024, (unsigned)lane * 16u, lds0 + (unsigned)(i * 9 + w) * 1024u);
  const char* wfp = ws + WS_WOF + (size_t)nt * 18432 + lane * 16;
  bf16x8 bfr[18];
#pragma unroll
  for (int ks = 0; ks < 18; ++ks) bfr[ks] = *(const bf16x8*)(wfp + ks * 1024);
  u16 xr[2][4];
#pragma unroll
  for (int t = 0; t < 2; ++t)
#pragma unroll
    for (int j = 0; j < 4; ++j) xr[t][j] = x[(size_t)(tt32 * 32 + t * 16 + g * 4 + j) * 576 + nt * 16 + c16];
  asm volatile("s_waitcnt vmcnt(26)\n\ts_barrier" ::: "memory");
  f32x4 acc[2] = {(f32x4){0, 0, 0, 0}, (f32x4){0, 0, 0, 0}};
#pragma unroll
  for (int ks = 0; ks < 18; ++ks) {
    acc[0] = MFMA16(ldsr128((lds_cptr)lds + ks * 1024 + lane * 16), bfr[ks], acc[0]);
    acc[1] = MFMA16(ldsr128((lds_cptr)lds + 18432 + ks * 1024 + lane * 16), bfr[ks], acc[1]);
  }
  float* hp = (float*)(ws + WS_H);
  float* pst = (float*)(lds + 36864);
  float* fin = (float*)(lds + 36864 + 9 * 256);
#pragma unroll
  for (int t = 0; t < 2; ++t)
#pragma unroll
    for (int j = 0; j < 4; ++j) {
      const float hv = acc[t][j] + bf2f(xr[t][j]);
      acc[t][j] = hv;
      hp[(size_t)(tt32 * 32 + t * 16 + g * 4 + j) * 576 + nt * 16 + c16] = hv;
      const float sm = row16_sum(hv);
      const float d = hv - sm * (1.f / 16.f);
      const float m2 = row16_sum(d * d);
      if (c16 == 0) { pst[(w * 32 + t * 16 + g * 4 + j) * 2] = sm; pst[(w * 32 + t * 16 + g * 4 + j) * 2 + 1] = m2; }
    }
  STAMP(2, 1);
  BARRIER();
  if (w == 0) {
    const int tok = lane & 31, st = lane >> 5;
    float sw[9], S = 0.f, M2 = 0.f;
#pragma unroll
    for (int i = 0; i < 9; ++i) { sw[i] = pst[(i * 32 + tok) * 2]; M2 += pst[(i * 32 + tok) * 2 + 1]; S += sw[i]; }
    const float mub = S * (1.f / 144.f);
#pragma unroll
    for (int i = 0; i < 9; ++i) { const float d = sw[i] * (1.f / 16.f) - mub; M2 += 16.f * d * d; }
    gu64* gr = (gu64*)(ws + WS_GRAN + (size_t)tt32 * 2048);
    __hip_atomic_store(gr + ct * 64 + lane, (1ull << 32) | (unsigned long long)__float_as_uint(st ? M2 : S), RLX_AGENT);
    float pv[4];
    for (unsigned spins = 0;; ++spins) {
      bool ok = true;
#pragma unroll
      for (int i = 0; i < 4; ++i) {
        const unsigned long long v = __hip_atomic_load(gr + i * 64 + lane, RLX_AGENT);
        pv[i] = __uint_as_float((unsigned)v);
        ok &= (v >> 32) == 1ull;
      }
      if (__all(ok) || spins > (1u << 22)) break;
      __builtin_amdgcn_s_sleep(1);
    }
    const float tot = (pv[0] + pv[1]) + (pv[2] + pv[3]);
    const float mu = tot * (1.f / 576.f);
    float extra = 0.f;
#pragma unroll
    for (int i = 0; i < 4; ++i) { const float d = pv[i] * (1.f / 144.f) - mu; extra += 144.f * d * d; }
    const float other = __shfl_xor(tot, 32);
    const float var = (other + extra) * (1.f / 576.f);
    const float r1 = rsqrtf(var + LN_EPS);
    const float r2 = rsqrtf(var * r1 * r1 + LN_EPS);
    if (st == 0) { fin[tok * 2] = mu; fin[tok * 2 + 1] = r1 * r2; }
  }
  BARRIER();
  STAMP(2, 2);
  char* hnp = ws + WS_HN;
#pragma unroll
  for (int t = 0; t < 2; ++t) {
    const f32x4 f0 = *(const f32x4*)(fin + (t * 16 + g * 4) * 2), f1 = *(const f32x4*)(fin + (t * 16 + g * 4) * 2 + 4);
    u32x2 v;
    v[0] = cvtpk((acc[t][0] - f0[0]) * f0[1], (acc[t][1] - f0[2]) * f0[3]);
    v[1] = cvtpk((acc[t][2] - f1[0]) * f1[1], (acc[t][3] - f1[2]) * f1[3]);
    *(u32x2*)(hnp + ((size_t)(nt * 64 + tt32) * 64 + (2 * t + (g >> 1)) * 16 + c16) * 16 + (g & 1) * 8) = v;
  }
  STAMP(2, 3);
}

__device__ __forceinline__ float gelu_tanh(float v) {
  const float z = v * (0.7978845608028654f + 0.035677408136300125f * v * v);
  const float e = __builtin_amdgcn_exp2f(z * -2.8853900817779268f);
  return v * __builtin_amdgcn_rcpf(1.0f + e);
}
constexpr int G1_ST = 5, G1_SB = 28672;
__global__ __launch_bounds__(512, 1) void k4_gemm1(const u16* __restrict__ w1, const u16* __restrict__ b1, char* __restrict__ ws) {
  __shared__ __attribute__((aligned(16))) char lds[G1_ST * G1_SB];
  const int tid = threadIdx.x, lane = tid & 63;
  const int w8 = __builtin_amdgcn_readfirstlane(tid >> 6), w = w8 & 3;
  const int id = (blockIdx.x & 7) * 27 + (blockIdx.x >> 3);
  const int pn = id / 6, pm = id - pn * 6;
  const unsigned lds0 = (unsigned)(uintptr_t)lds;
  STAMP(3, 0);
  if (w8 >= 4) {
    const char* hn = ws + WS_HN + (size_t)(pm * 6) * 65536;
    unsigned aoff[3];
#pragma unroll
    for (int i = 0; i < 3; ++i) { const int a = w + 4 * i; aoff[i] = (unsigned)((a >> 1) * 65536 + (a & 1) * 1024 + lane * 16); }
    const char* wb = (const char*)w1 + (size_t)pn * 256;
    unsigned boff[4];
#pragma unroll
    for (int i = 0; i < 4; ++i) {
      const int row = (4 * w + i) * 4 + (lane >> 4), cp = lane & 15;
      const int f = (row & 3) | (((row >> 3) & 1) << 2);
      const int ch = (((cp >> 1) ^ f) << 1) | (cp & 1);
      boff[i] = (unsigned)(row * 9216 + ch * 16);
    }
#define G1_ISSUE(t, slot) do { const unsigned sl_ = lds0 + (unsigned)(slot) * G1_SB; \
    const char* ab_ = hn + (size_t)(t) * 2048; const char* bb_ = wb + (size_t)(t) * (64 * 9216); \
    _Pragma("unroll") for (int i = 0; i < 4; ++i) glds16(bb_, boff[i], sl_ + 12288u + (unsigned)(4 * w + i) * 1024u); \
    _Pragma("unroll") for (int i = 0; i < 3; ++i) glds16(ab_, aoff[i], sl_ + (unsigned)(w + 4 * i) * 1024u); } while (0)
#pragma unroll
    for (int t = 0; t < G1_ST - 1; ++t) G1_ISSUE(t, t);
    int islot = G1_ST - 1;
    for (int t = 0; t < 32 - (G1_ST - 1); ++t) {
      asm volatile("s_waitcnt vmcnt(21)\n\ts_barrier" ::: "memory");
      G1_ISSUE(t + G1_ST - 1, islot);
      islot = (islot + 1 == G1_ST) ? 0 : islot + 1;
    }
    asm volatile("s_waitcnt vmcnt(21)\n\ts_barrier" ::: "memory");
    asm volatile("s_waitcnt vmcnt(14)\n\ts_barrier" ::: "memory");
    asm volatile("s_waitcnt vmcnt(7)\n\ts_barrier" ::: "memory");
    asm volatile("s_waitcnt vmcnt(0)\n\ts_barrier" ::: "memory");
    return;
  }
  const int wr = w >> 1, wc = w & 1;
  const int g = lane >> 4, c16 = lane & 15, q4 = (lane & 15) >> 2, p4 = lane & 3;
  const int fl = q4 | ((g & 1) << 2);
  unsigned brd[4];
#pragma unroll
  for (int n = 0; n < 4; ++n) brd[n] = (unsigned)(12288 + (8 * g + q4) * 256 + (((wc * 4 + n) ^ fl) * 32) + 8 * p4);
  const unsigned ard = (unsigned)(wr * 6144 + lane * 16);
  f32x4 acc[4][3];
#pragma unroll
  for (int n = 0; n < 4; ++n)
#pragma unroll
    for (int m = 0; m < 3; ++m) acc[n][m] = (f32x4){0, 0, 0, 0};
  int slot = 0;
  bf16x8 aX[3], bX[4], aY[3], bY[4];
#define G1_READ(A_, B_, ks) do { const lds_cptr sp_ = (lds_cptr)lds + slot * G1_SB; \
    _Pragma("unroll") for (int n = 0; n < 4; ++n) B_[n] = cat8(trread(sp_ + brd[n] + (ks) * 8192), trread(sp_ + brd[n] + (ks) * 8192 + 1024)); \
    _Pragma("unroll") for (int m = 0; m < 3; ++m) A_[m] = ldsr128(sp_ + ard + m * 2048 + (ks) * 1024); } while (0)
#define G1_MMA(A_, B_) do { _Pragma("unroll") for (int n = 0; n < 4; ++n) _Pragma("unroll") for (int m = 0; m < 3; ++m) \
    acc[n][m] = MFMA16(B_[n], A_[m], acc[n][m]); } while (0)
#define G1_ILV() do { _Pragma("unroll") for (int i_ = 0; i_ < 6; ++i_) { SGB(0x8, 1); SGB(0x100, 2); } SGB(0x8, 6); } while (0)
  asm volatile("" :: "s"(b1), "s"(ws));
  BARRIER();
  STAMP(3, 1);
  __builtin_amdgcn_s_waitcnt(0xC07F);
  G1_READ(aX, bX, 0);
  SB0();
  for (int t = 0; t < 31; ++t) {
    G1_READ(aY, bY, 1); G1_MMA(aX, bX); G1_ILV(); SB0();
    BARRIER();
    slot = (slot + 1 == G1_ST) ? 0 : slot + 1;
    SB0();
    G1_READ(aX, bX, 0); G1_MMA(aY, bY); G1_ILV(); SB0();
  }
  G1_READ(aY, bY, 1); G1_MMA(aX, bX); G1_ILV(); SB0();
  G1_MMA(aY, bY);
  STAMP(3, 3);
  char* up = ws + WS_U;
#pragma unroll
  for (int n = 0; n < 4; ++n) {
    const int ft = pn * 8 + wc * 4 + n;
    const u32x2 bv = *(const u32x2*)(b1 + ft * 16 + g * 4);
    const float bb0 = __uint_as_float(bv[0] << 16), bb1 = __uint_as_float(bv[0] & 0xffff0000u);
    const float bb2 = __uint_as_float(bv[1] << 16), bb3 = __uint_as_float(bv[1] & 0xffff0000u);
#pragma unroll
    for (int m = 0; m < 3; ++m) {
      const int dt = pm * 6 + wr * 3 + m;
      u32x2 v;
      v[0] = cvtpk(gelu_tanh(acc[n][m][0] + bb0), gelu_tanh(acc[n][m][1] + bb1));
      v[1] = cvtpk(gelu_tanh(acc[n][m][2] + bb2), gelu_tanh(acc[n][m][3] + bb3));
      *(u32x2*)(up + ((size_t)(dt * 144 + (ft >> 1)) * 64 + ((ft & 1) * 2 + (g >> 1)) * 16 + c16) * 16 + (g & 1) * 8) = v;
    }
  }
  STAMP(3, 4);
#undef G1_ISSUE
#undef G1_READ
#undef G1_MMA
}

constexpr int G2_ST = 4, G2_SB = 36864;
__global__ __launch_bounds__(512, 1) void k5_gemm2(const u16* __restrict__ w2, const u16* __restrict__ b2, char* __restrict__ ws,
                                                    u16* __restrict__ out) {
  __shared__ __attribute__((aligned(16))) char lds[G2_ST * G2_SB];
  const int tid = threadIdx.x, lane = tid & 63;
  const int w8 = __builtin_amdgcn_readfirstlane(tid >> 6), w = w8 & 3;
  const int xg = blockIdx.x & 7, o = blockIdx.x >> 3;
  const int kq = o & 3, pm = (o >> 2) & 3, pn = xg * 2 + (o >> 4);
  const int tile = pn * 4 + pm;
  const unsigned lds0 = (unsigned)(uintptr_t)lds;
  STAMP(4, 0);
  if (w8 >= 4) {
    const char* ub = ws + WS_U + (size_t)(pm * 9) * 147456 + (size_t)kq * 36864;
    unsigned aoff[5];
#pragma unroll
    for (int i = 0; i < 5; ++i) { int a = w + 4 * i; if (a >= 18) a -= 4; aoff[i] = (unsigned)((a >> 1) * 147456 + (a & 1) * 1024 + lane * 16); }
    const char* wb = (const char*)w2 + (size_t)kq * 1152 * 4096 + (size_t)pn * 256;
    unsigned boff[4];
#pragma unroll
    for (int i = 0; i < 4; ++i) {
      const int row = (4 * w + i) * 4 + (lane >> 4), cp = lane & 15;
      const int f = (row & 3) | (((row >> 3) & 1) << 2);
      const int ch = (((cp >> 1) ^ f) << 1) | (cp & 1);
      boff[i] = (unsigned)(row * 4096 + ch * 16);
    }
#define G2_ISSUE(NA, t, slot) do { const unsigned sl_ = lds0 + (unsigned)(slot) * G2_SB; \
    const char* ab_ = ub + (size_t)(t) * 2048; const char* bb_ = wb + (size_t)(t) * (64 * 4096); \
    _Pragma("unroll") for (int i = 0; i < 4; ++i) glds16(bb_, boff[i], sl_ + 20480u + (unsigned)(4 * w + i) * 1024u); \
    _Pragma("unroll") for (int i = 0; i < NA; ++i) glds16(ab_, aoff[i], sl_ + (unsigned)(w + 4 * i) * 1024u); } while (0)
#define G2_LOADER(NA, W2_, W1_) do { \
    _Pragma("unroll") for (int t = 0; t < G2_ST - 1; ++t) G2_ISSUE(NA, t, t); \
    int islot = G2_ST - 1; \
    for (int t = 0; t < 18 - (G2_ST - 1); ++t) { \
      asm volatile("s_waitcnt vmcnt(" #W2_ ")\n\ts_barrier" ::: "memory"); \
      G2_ISSUE(NA, t + G2_ST - 1, islot); \
      islot = (islot + 1 == G2_ST) ? 0 : islot + 1; } \
    asm volatile("s_waitcnt vmcnt(" #W2_ ")\n\ts_barrier" ::: "memory"); \
    asm volatile("s_waitcnt vmcnt(" #W1_ ")\n\ts_barrier" ::: "memory"); \
    asm volatile("s_waitcnt vmcnt(0)\n\ts_barrier" ::: "memory"); } while (0)
    if (w < 2) G2_LOADER(5, 18, 9); else G2_LOADER(4, 16, 8);
    return;
  }
  const int g = lane >> 4, c16 = lane & 15, q4 = (lane & 15) >> 2, p4 = lane & 3;
  const int fl = q4 | ((g & 1) << 2);
  unsigned brd[2];
#pragma unroll
  for (int n = 0; n < 2; ++n) brd[n] = (unsigned)(20480 + (8 * g + q4) * 256 + (((w * 2 + n) ^ fl) * 32) + 8 * p4);
  const unsigned ard = (unsigned)(lane * 16);
  f32x4 acc[18];
#pragma unroll
  for (int i = 0; i < 18; ++i) acc[i] = (f32x4){0, 0, 0, 0};
  int slot = 0;
  bf16x8 aX[9], bX[2], aY[9], bY[2];
#define G2_READ(A_, B_, ks) do { const lds_cptr sp_ = (lds_cptr)lds + slot * G2_SB; \
    _Pragma("unroll") for (int n = 0; n < 2; ++n) B_[n] = cat8(trread(sp_ + brd[n] + (ks) * 8192), trread(sp_ + brd[n] + (ks) * 8192 + 1024)); \
    _Pragma("unroll") for (int m = 0; m < 9; ++m) A_[m] = ldsr128(sp_ + ard + m * 2048 + (ks) * 1024); } while (0)
#define G2_MMA(A_, B_) do { _Pragma("unroll") for (int m = 0; m < 9; ++m) { \
    acc[m * 2] = MFMA16(A_[m], B_[0], acc[m * 2]); acc[m * 2 + 1] = MFMA16(A_[m], B_[1], acc[m * 2 + 1]); } } while (0)
#define G2_ILV() do { _Pragma("unroll") for (int i_ = 0; i_ < 6; ++i_) { SGB(0x8, 1); SGB(0x100, 2); } SGB(0x8, 1); SGB(0x100, 1); SGB(0x8, 11); } while (0)
  asm volatile("" :: "s"(b2), "s"(ws), "s"(out));
  BARRIER();
  STAMP(4, 1);
  __builtin_amdgcn_s_waitcnt(0xC07F);
  G2_READ(aX, bX, 0);
  SB0();
  for (int t = 0; t < 17; ++t) {
    G2_READ(aY, bY, 1); G2_MMA(aX, bX); G2_ILV(); SB0();
    BARRIER();
    slot = (slot + 1 == G2_ST) ? 0 : slot + 1;
    SB0();
    G2_READ(aX, bX, 0); G2_MMA(aY, bY); G2_ILV(); SB0();
  }
  G2_READ(aY, bY, 1); G2_MMA(aX, bX); G2_ILV(); SB0();
  G2_MMA(aY, bY);
  STAMP(4, 3);
  const auto rsrc = __builtin_amdgcn_make_buffer_rsrc(ws + WS_SLAB + (size_t)tile * SLABT, 0, SLABT, 0x00020000);
#define PKW(i, e) __builtin_elementwise_min(cvtpk(acc[i][2 * (e)], acc[i][2 * (e) + 1]), 0xFFFFFFFEu)
#pragma unroll
  for (int dst = 0; dst < 4; ++dst) {
    if (dst != kq) {
      const int sb = (dst * 4 + kq) * 10240 + w * 2560;
      __builtin_amdgcn_raw_buffer_store_b128((u32x4){PKW(dst, 0), PKW(dst, 1), PKW(dst + 4, 0), PKW(dst + 4, 1)}, rsrc, sb + lane * 16, 0, 16);
      __builtin_amdgcn_raw_buffer_store_b128((u32x4){PKW(dst + 8, 0), PKW(dst + 8, 1), PKW(dst + 12, 0), PKW(dst + 12, 1)}, rsrc, sb + 1024 + lane * 16, 0, 16);
      if (dst < 2) __builtin_amdgcn_raw_buffer_store_b64((u32x2){PKW((dst + 16) % 18, 0), PKW((dst + 16) % 18, 1)}, rsrc, sb + 2048 + lane * 8, 0, 16);
    }
  }
#undef PKW
  STAMP(4, 4);
  const float* hp = (const float*)(ws + WS_H);
  f32x4 own[5]; unsigned oo[5];
#pragma unroll
  for (int i4 = 0; i4 < 5; ++i4) {
    const int ie = (i4 == 4 && kq >= 2) ? 3 : i4;
    const int tau = ie * 4 + kq, m = tau >> 1, n = tau & 1;
    const int sp_ = pn * 128 + (w * 2 + n) * 16 + c16, d0 = pm * 144 + m * 16 + g * 4;
    oo[i4] = (unsigned)(sp_ * 576 + d0);
    const f32x4 hv = *(const f32x4*)(hp + oo[i4]);
    const float bb = bf2f(b2[sp_]);
    own[i4] = (kq == 0) ? acc[i4 * 4] : (kq == 1) ? acc[i4 * 4 + 1] : (kq == 2) ? acc[(i4 * 4 + 2) % 18] : acc[(i4 * 4 + 3) % 18];
    own[i4] += hv + bb;
  }
  u32x4 pa[3], pb[3]; u32x2 pc[3];
  const int x8 = (kq < 2) ? 2048 + lane * 8 : lane * 16;
  for (unsigned spins = 0;; ++spins) {
#pragma unroll
    for (int s = 0; s < 3; ++s) {
      const int sb = (kq * 4 + ((kq + s + 1) & 3)) * 10240 + w * 2560;
      pa[s] = __builtin_amdgcn_raw_buffer_load_b128(rsrc, sb + lane * 16, 0, 16);
      pb[s] = __builtin_amdgcn_raw_buffer_load_b128(rsrc, sb + 1024 + lane * 16, 0, 16);
      pc[s] = __builtin_amdgcn_raw_buffer_load_b64(rsrc, sb + x8, 0, 16);
    }
    unsigned mx = 0;
#pragma unroll
    for (int s = 0; s < 3; ++s) {
#pragma unroll
      for (int e = 0; e < 4; ++e) mx = max(mx, max(pa[s][e], pb[s][e]));
      mx = max(mx, max(pc[s][0], pc[s][1]));
    }
    if (__all(mx != 0xFFFFFFFFu) || spins > (1u << 20)) break;
    __builtin_amdgcn_s_sleep(2);
  }
  STAMP(4, 5);
#define UPK(d) (f32x4){__uint_as_float((d)[0] << 16), __uint_as_float((d)[0] & 0xffff0000u), __uint_as_float((d)[1] << 16), __uint_as_float((d)[1] & 0xffff0000u)}
#pragma unroll
  for (int i4 = 0; i4 < 5; ++i4) {
    f32x4 a = own[i4];
#pragma unroll
    for (int s = 0; s < 3; ++s) {
      const u32x2 d = (i4 == 0) ? (u32x2){pa[s][0], pa[s][1]} : (i4 == 1) ? (u32x2){pa[s][2], pa[s][3]} :
                      (i4 == 2) ? (u32x2){pb[s][0], pb[s][1]} : (i4 == 3) ? (u32x2){pb[s][2], pb[s][3]} : pc[s];
      a += UPK(d);
    }
    if (i4 < 4 || kq < 2) *(u32x2*)(out + oo[i4]) = (u32x2){cvtpk(a[0], a[1]), cvtpk(a[2], a[3])};
  }
#undef UPK
  STAMP(4, 6);
#undef G2_ISSUE
#undef G2_READ
#undef G2_MMA
#undef G2_LOADER
}

extern "C" void kernel_launch(void* const* d_in, const int* in_sizes, int n_in,
                              void* d_out, int out_size, void* d_ws, size_t ws_size,
                              hipStream_t stream) {
  const u16* x    = (const u16*)d_in[0];
  const u16* wqkv = (const u16*)d_in[1];
  const u16* wo   = (const u16*)d_in[2];
  const u16* w1   = (const u16*)d_in[4];
  const u16* b1   = (const u16*)d_in[5];
  const u16* w2   = (const u16*)d_in[6];
  const u16* b2   = (const u16*)d_in[7];
  char* ws = (char*)d_ws;
  k1_qkv<<<256, 512, 0, stream>>>(x, wqkv, ws);
  k2_attn<<<256, 512, 0, stream>>>(ws, wo, w1, w2);
  k3_proj<<<256, 576, 0, stream>>>(x, ws);
  k4_gemm1<<<216, 512, 0, stream>>>(w1, b1, ws);
  k5_gemm2<<<256, 512, 0, stream>>>(w2, b2, ws, (u16*)d_out);
}
```

```cpp
#include <hip/hip_runtime.h>
#include <stdint.h>

typedef short    bf16x8 __attribute__((ext_vector_type(8)));
typedef short    s16x4  __attribute__((ext_vector_type(4)));
typedef float    f32x4  __attribute__((ext_vector_type(4)));
typedef float    f32x16 __attribute__((ext_vector_type(16)));
typedef unsigned u32x2  __attribute__((ext_vector_type(2)));
typedef unsigned u32x4  __attribute__((ext_vector_type(4)));
typedef __bf16   bf16x2_t __attribute__((ext_vector_type(2)));
typedef unsigned short u16;
#define LDS_AS __attribute__((address_space(3)))
#define GLB_AS __attribute__((address_space(1)))
typedef LDS_AS const char* lds_cptr;
typedef GLB_AS unsigned gu32;
typedef GLB_AS unsigned long long gu64;
#define RLX_AGENT __ATOMIC_RELAXED, __HIP_MEMORY_SCOPE_AGENT

constexpr int S_ = 2048, D_ = 576, F_ = 4608;
constexpr float LN_EPS = 1e-5f;
constexpr float QSCALE = 0.23570226039551584f * 1.4426950408889634f;

constexpr unsigned VREC    = 640;
constexpr unsigned KXOFF   = 65536 + 128 * VREC;
constexpr unsigned KVHEAD  = KXOFF + 8192;
constexpr size_t WS_QM   = 0;
constexpr size_t WS_QX   = WS_QM + 32u * 65536;
constexpr size_t WS_KV   = WS_QX + 32u * 8192;
constexpr size_t WS_OF   = WS_KV + 32u * KVHEAD;
constexpr size_t WS_WOF  = WS_OF + 128u * 18 * 1024;
constexpr size_t WS_HN   = WS_WOF + 36u * 18 * 1024;
constexpr size_t WS_H    = WS_HN + 36u * 64 * 1024;
constexpr size_t WS_U    = WS_H + 2048u * 576 * 4;
constexpr size_t WS_GRAN = WS_U + 36u * 144 * 1024;
constexpr size_t WS_SLAB = WS_GRAN + 64u * 4 * 512;
constexpr unsigned SLABT = 16u * 10240;

__device__ __forceinline__ unsigned cvtpk(float lo, float hi) {
  typedef float f32x2_t __attribute__((ext_vector_type(2)));
  return __builtin_bit_cast(unsigned, __builtin_convertvector((f32x2_t){lo, hi}, bf16x2_t));
}
__device__ __forceinline__ float bf2f(u16 v) { return __uint_as_float((unsigned)v << 16); }
__device__ __forceinline__ void glds16(const void* sbase, unsigned voff, unsigned lds_base) {
  unsigned sv;
  asm volatile("s_mov_b32 %0, m0\n\ts_mov_b32 m0, %3\n\ts_nop 0\n\tglobal_load_lds_dwordx4 %1, %2\n\ts_mov_b32 m0, %0"
               : "=&s"(sv) : "v"(voff), "s"(sbase), "s"(lds_base) : "memory");
}
__device__ __forceinline__ void touch4(const void* sbase, unsigned voff, unsigned lds_base) {
  unsigned sv;
  asm volatile("s_mov_b32 %0, m0\n\ts_mov_b32 m0, %3\n\ts_nop 0\n\tglobal_load_lds_dword %1, %2\n\ts_mov_b32 m0, %0"
               : "=&s"(sv) : "v"(voff), "s"(sbase), "s"(lds_base) : "memory");
}
__device__ __forceinline__ s16x4 trread(lds_cptr p) {
  typedef short v4i16_t __attribute__((ext_vector_type(4)));
  return __builtin_bit_cast(s16x4, __builtin_amdgcn_ds_read_tr16_b64_v4i16((LDS_AS v4i16_t*)p));
}
__device__ __forceinline__ bf16x8 cat8(s16x4 a, s16x4 b) {
  return (bf16x8){a[0], a[1], a[2], a[3], b[0], b[1], b[2], b[3]};
}
__device__ __forceinline__ bf16x8 ldsr128(lds_cptr p) { return *(const LDS_AS bf16x8*)p; }
#define MFMA16(a, b, c) __builtin_amdgcn_mfma_f32_16x16x32_bf16((a), (b), (c), 0, 0, 0)
#define MFMA32(a, b, c) __builtin_amdgcn_mfma_f32_32x32x16_bf16((a), (b), (c), 0, 0, 0)
#define SB0() __builtin_amdgcn_sched_barrier(0)
#define SGB(mask, n) __builtin_amdgcn_sched_group_barrier((mask), (n), 0)
#define WAITV(n) asm volatile("s_waitcnt vmcnt(" #n ")" ::: "memory")
#define BARRIER() do { asm volatile("" ::: "memory"); __builtin_amdgcn_s_waitcnt(0xC07F); __builtin_amdgcn_s_barrier(); asm volatile("" ::: "memory"); } while (0)
__device__ __forceinline__ float dot2(unsigned a, unsigned b, float c) {
  return __builtin_amdgcn_fdot2_f32_bf16(__builtin_bit_cast(bf16x2_t, a), __builtin_bit_cast(bf16x2_t, b), c, false);
}
constexpr unsigned ONES2 = 0x3F803F80u;
__device__ __forceinline__ float sel4(float a, float b, float c, float d, unsigned m0, unsigned m1, unsigned m2, unsigned m3) {
  return __uint_as_float((__float_as_uint(a) & m0) | (__float_as_uint(b) & m1) | (__float_as_uint(c) & m2) | (__float_as_uint(d) & m3));
}
__device__ __forceinline__ unsigned eqmask(int a, int b) { unsigned m = (a == b) ? ~0u : 0u; asm volatile("" : "+v"(m)); return m; }
#define STAMP(kid, i) do {} while (0)
#define TSTAMP(i) do {} while (0)
#define TFLUSH(kid, a, b) do {} while (0)

#define CODE_ADDR(dst, sym) do { unsigned lo_, hi_; \
    asm volatile("s_getpc_b64 s[98:99]\n\ts_add_u32 %0, s98, " sym "@rel32@lo+4\n\ts_addc_u32 %1, s99, " sym "@rel32@hi+12" \
                 : "=s"(lo_), "=s"(hi_) :: "s98", "s99", "scc"); \
    dst = (const char*)(((unsigned long long)hi_ << 32) | lo_); } while (0)

constexpr int Q_ST = 5, Q_SB = 30720;
__global__ __launch_bounds__(512, 1) void k1_qkv(const u16* __restrict__ x, const u16* __restrict__ wqkv, const u16* __restrict__ wo,
                                                  char* __restrict__ ws) {
  __shared__ __attribute__((aligned(16))) char lds[Q_ST * Q_SB + 512 + 1024];
  const int tid = threadIdx.x, lane = tid & 63;
  const int w8 = __builtin_amdgcn_readfirstlane(tid >> 6), w = w8 & 3;
  const int b = blockIdx.x, xg = b & 7, o = b >> 3;
  const int bm = (xg >> 1) * 4 + (o & 3), hg = (xg & 1) * 8 + (o >> 2);
  const unsigned lds0 = (unsigned)(uintptr_t)lds;
  float* csl = (float*)(lds + Q_ST * Q_SB);
  constexpr unsigned DUMMY = Q_ST * Q_SB + 512;
  constexpr int SP = 304;
  u16 wog[8];
  STAMP(0, 0);
  if (w8 >= 4) {
    const char* xb = (const char*)x + (size_t)bm * 128 * 1152;
    const char* wb = (const char*)wqkv + (216 * hg - 8 * (hg & 1));
    unsigned xoff[4], woff[4];
#pragma unroll
    for (int i = 0; i < 4; ++i) {
      const int row = (w + 4 * i) * 8 + (lane >> 3);
      xoff[i] = (unsigned)(row * 1152 + (((lane & 7) ^ (lane >> 3)) * 16));
      int pc = w + 4 * i; if (pc >= 14) pc -= 4;
      const unsigned c = (unsigned)(pc * 64 + lane), pos = c / 14u, ch = c - pos * 14u;
      const unsigned k = (pos & ~12u) | ((pos & 4u) << 1) | ((pos & 8u) >> 1);
      woff[i] = k * 3456u + ch * 16u;
    }
    int it_ = 0; unsigned isl = lds0;
#define Q_ISSUE() do { const char* xb_ = xb + (size_t)it_ * 128; const char* wb_ = wb + (size_t)it_ * (64 * 3456); \
    _Pragma("unroll") for (int i = 0; i < 3; ++i) glds16(wb_, woff[i], isl + 16384u + (unsigned)(w + 4 * i) * 1024u); \
    if (w < 2) glds16(wb_, woff[3], isl + 16384u + (unsigned)(w + 12) * 1024u); \
    _Pragma("unroll") for (int i = 0; i < 4; ++i) glds16(xb_, xoff[i], isl + (unsigned)(w + 4 * i) * 1024u); \
    ++it_; isl = (isl + Q_SB == lds0 + Q_ST * Q_SB) ? lds0 : isl + Q_SB; } while (0)
#define Q_WAIT(NA, NB) do { if (w < 2) asm volatile("s_waitcnt vmcnt(" #NA ")\n\ts_barrier" ::: "memory"); \
                            else       asm volatile("s_waitcnt vmcnt(" #NB ")\n\ts_barrier" ::: "memory"); } while (0)
    Q_ISSUE(); Q_ISSUE(); Q_ISSUE();
    Q_WAIT(16, 14); Q_ISSUE(); Q_ISSUE();
#pragma unroll 1
    for (int t = 1; t < 5; ++t) { Q_WAIT(24, 21); Q_ISSUE(); }
    {
      const int tk = b * 162 + ((tid - 256 < 162) ? tid - 256 : 0);
      const int l_ = tk & 63, ks = (tk >> 6) % 18, nt = tk / (64 * 18);
      const int g_ = l_ >> 4, n_ = nt * 16 + (l_ & 15);
      const int k0 = (ks < 16) ? 18 * (2 * ks + (g_ >> 1)) + 4 * (g_ & 1) : 18 * (16 * (ks - 16) + 4 * g_) + 16;
      const u16* wp_ = wo + k0 * 576 + n_;
#pragma unroll
      for (int j = 0; j < 8; ++j) {
        const int dk = (ks < 16) ? (j & 3) + 8 * (j >> 2) : 18 * (j >> 1) + (j & 1);
        wog[j] = wp_[dk * 576];
      }
    }
    Q_WAIT(32, 29);
    Q_WAIT(24, 22);
    Q_WAIT(16, 15);
    Q_WAIT(8, 8);
    if (tid - 256 < 32) *(u32x4*)(ws + WS_GRAN + (size_t)b * 512 + (tid - 256) * 16) = (u32x4){0, 0, 0, 0};
    BARRIER();
    BARRIER();
  } else {
    const unsigned vw = (unsigned)(o * 4 + w);
    {
      const char* c1; const char* c3;
      CODE_ADDR(c1, "_Z6k1_qkvPKtS0_S0_Pc"); CODE_ADDR(c3, "_Z7k3_projPKtPc");
      const unsigned nsec = (unsigned)(c3 - c1) >> 6;
      const unsigned ci = (vw & 7u) * 64u + lane;
      if ((vw & 7u) * 64u < nsec) touch4(c1, (ci < nsec ? ci : 0u) * 64u, lds0 + DUMMY);
    }
    const int g = lane >> 4, c16 = lane & 15, q4 = (lane & 15) >> 2, p4 = lane & 3;
#define Q_TILE(i) ((w == 3) ? ((i) == 0 ? 6 : (i) - 1) : ((i) < 2 ? 2 * w + (i) : ((i) < 2 + 2 * w ? (i) - 2 : (i))))
    unsigned wrd[7], so[7][2]; float qs[7][2];
#pragma unroll
    for (int i = 0; i < 7; ++i) {
      const int n = Q_TILE(i);
      int Q = 4 * n + p4; if (Q > 26) Q = 26;
      wrd[i] = (unsigned)(16384 + ((g >> 1) * 16 + (g & 1) * 4 + q4) * 224 + 8 * Q + 8 * (hg & 1));
#pragma unroll
      for (int e = 0; e < 2; ++e) {
        const unsigned col = (unsigned)(n * 16 + g * 4 + e * 2);
        const unsigned hl = col / 18u;
        so[i][e] = (unsigned)((w * 32 + c16) * SP) + col * 2u + hl * 12u;
        qs[i][e] = ((unsigned)hg * 6u + hl < 32u) ? QSCALE : 1.0f;
        asm volatile("" : "+v"(so[i][e]), "+v"(qs[i][e]));
      }
    }
    const unsigned xrd0 = (unsigned)((w * 32 + c16) * 128 + ((g ^ (c16 & 7)) * 16)), xrd1 = xrd0 ^ 64u;
    f32x4 acc[7][2], csa[2], sq[2], sx[2];
#pragma unroll
    for (int n = 0; n < 7; ++n) { acc[n][0] = (f32x4){0, 0, 0, 0}; acc[n][1] = (f32x4){0, 0, 0, 0}; }
#pragma unroll
    for (int i = 0; i < 2; ++i) { csa[i] = (f32x4){0, 0, 0, 0}; sq[i] = (f32x4){0, 0, 0, 0}; sx[i] = (f32x4){0, 0, 0, 0}; }
    const bf16x8 ones8 = __builtin_bit_cast(bf16x8, (u32x4){ONES2, ONES2, ONES2, ONES2});
    bf16x8 wX[7], xX[2], wY[7], xY[2];
    int slot = 0;
#define Q_READ(W_, X_, ks) do { const lds_cptr sp_ = (lds_cptr)lds + slot * Q_SB; \
    X_[0] = ldsr128(sp_ + ((ks) ? xrd1 : xrd0)); X_[1] = ldsr128(sp_ + ((ks) ? xrd1 : xrd0) + 2048); \
    _Pragma("unroll") for (int n = 0; n < 7; ++n) W_[n] = cat8(trread(sp_ + wrd[n] + (ks) * 7168), trread(sp_ + wrd[n] + (ks) * 7168 + 1792)); } while (0)
#define Q_MMA(W_, X_) do { \
    sx[0] = MFMA16(ones8, X_[0], sx[0]); sx[1] = MFMA16(ones8, X_[1], sx[1]); \
    sq[0] = MFMA16(X_[0], X_[0], sq[0]); sq[1] = MFMA16(X_[1], X_[1], sq[1]); \
    _Pragma("unroll") for (int n = 0; n < 7; ++n) { acc[n][0] = MFMA16(W_[n], X_[0], acc[n][0]); acc[n][1] = MFMA16(W_[n], X_[1], acc[n][1]); \
      if (n < 2) csa[n] = MFMA16(W_[n], ones8, csa[n]); } } while (0)
#define Q_ILV() do { _Pragma("unroll") for (int i_ = 0; i_ < 16; ++i_) { SGB(0x8, 1); SGB(0x100, 1); } SGB(0x8, 4); } while (0)
#define TDELAY 16
    {
      __builtin_amdgcn_s_sleep(TDELAY);
      unsigned i = vw * 64u + lane, rw = i / 27u;
      const char* wq = (const char*)wqkv + 192 * 3456 + (xg & 1) * 1728;
      touch4(wq, rw * 3456u + (i - rw * 27u) * 64u, lds0 + DUMMY);
      if (vw < 96u) { rw = i / 12u; touch4((const char*)x + (size_t)(xg >> 1) * 589824 + 384, rw * 1152u + (i - rw * 12u) * 64u, lds0 + DUMMY); }
      else { i += 2048u; rw = i / 27u; touch4(wq, rw * 3456u + (i - rw * 27u) * 64u, lds0 + DUMMY); }
      if (vw < 2u) { i = 10240u + vw * 64u + lane; rw = i / 27u; touch4(wq, rw * 3456u + (i - rw * 27u) * 64u, lds0 + DUMMY); }
    }
    asm volatile("" :: "s"(ws));
    BARRIER();
    STAMP(0, 1);
    __builtin_amdgcn_s_waitcnt(0xC07F);
    Q_READ(wX, xX, 0);
    SB0();
#pragma unroll
    for (int t = 0; t < 9; ++t) {
      Q_READ(wY, xY, 1); Q_MMA(wX, xX); Q_ILV(); SB0();
      if (t < 8) {
        BARRIER(); slot = (slot + 1 == Q_ST) ? 0 : slot + 1;
        SB0();
        Q_READ(wX, xX, 0); Q_MMA(wY, xY); Q_ILV(); SB0();
      } else { Q_MMA(wY, xY); }
    }
    STAMP(0, 2);
    if (c16 == 0) { *(f32x4*)(csl + (2 * w) * 16 + g * 4) = csa[0]; if (w < 3) *(f32x4*)(csl + (2 * w + 1) * 16 + g * 4) = csa[1]; }
    float rstd[2], mr[2];
    const unsigned jm0 = eqmask(c16 & 3, 0), jm1 = eqmask(c16 & 3, 1), jm2 = eqmask(c16 & 3, 2), jm3 = eqmask(c16 & 3, 3);
#pragma unroll
    for (int tt = 0; tt < 2; ++tt) {
      const float dg = sel4(sq[tt][0], sq[tt][1], sq[tt][2], sq[tt][3], jm0, jm1, jm2, jm3);
      const float ss = __shfl(dg, (c16 >> 2) * 16 + c16);
      const float mean = sx[tt][0] * (1.f / 576.f);
      rstd[tt] = rsqrtf(fmaxf(ss * (1.f / 576.f) - mean * mean, 0.f) + LN_EPS);
      mr[tt] = -mean;
    }
    BARRIER();
#pragma unroll
    for (int n = 0; n < 7; ++n) {
      const int nt = Q_TILE(n);
      const f32x4 cs4 = *(const f32x4*)(csl + nt * 16 + g * 4);
#pragma unroll
      for (int e = 0; e < 2; ++e)
#pragma unroll
        for (int tt = 0; tt < 2; ++tt) {
          const float rs = rstd[tt] * qs[n][e];
          const float v0 = __builtin_fmaf(mr[tt], cs4[2 * e], acc[n][tt][2 * e]) * rs;
          const float v1 = __builtin_fmaf(mr[tt], cs4[2 * e + 1], acc[n][tt][2 * e + 1]) * rs;
          *(unsigned*)(lds + so[n][e] + tt * 16 * SP) = cvtpk(v0, v1);
        }
    }
    BARRIER();
  }
  STAMP(0, 3);
  {
    const unsigned t8 = (unsigned)tid & 255u;
    const unsigned Tl = t8 >> 1, c = t8 & 1u, T = (unsigned)bm * 128u + Tl;
    const unsigned oQK = (T >> 5) * 1024u + c * 512u + (T & 31u) * 16u;
    const unsigned T2 = (unsigned)bm * 128u + (t8 & 127u);
    const unsigned oKX = KXOFF + (T2 >> 7) * 512u + (T2 & 31u) * 16u + ((T2 >> 5) & 3u) * 4u;
    const unsigned vrec = t8 >> 5, vhh = (t8 >> 4) & 1u, vi = t8 & 15u;
    const lds_cptr vsrc = (lds_cptr)lds + ((vrec * 16u + vhh * 4u + (vi >> 2)) * SP + (vi & 3u) * 8u);
    const lds_cptr vsrx = (lds_cptr)lds + ((vrec * 16u + vhh * 4u + (vi >> 2)) * SP + 32u);
    const unsigned oV = 65536u + ((unsigned)bm * 8u + vrec) * VREC + vhh * 320u + vi * 16u;
#pragma unroll
    for (int i = 0; i < 3; ++i) {
      const int hl = (w8 >> 2) * 3 + i;
      const int hs = hg * 6 + hl, which = hs >> 5, h = hs & 31;
      char* base = ws + (which == 0 ? WS_QM + (size_t)h * 65536 : WS_KV + (size_t)h * KVHEAD);
      if (which == 2) {
        const bf16x8 vm = cat8(trread(vsrc + hl * 48), trread(vsrc + hl * 48 + 8 * SP));
        bf16x8 vx = cat8(trread(vsrx + hl * 48), trread(vsrx + hl * 48 + 8 * SP));
        *(bf16x8*)(base + oV) = vm;
        if (vi == 2u) vx = __builtin_bit_cast(bf16x8, (u32x4){ONES2, ONES2, ONES2, ONES2});
        if (vi == 3u) vx = (bf16x8){0, 0, 0, 0, 0, 0, 0, 0};
        if (vi < 4u) *(bf16x8*)(base + oV + 256u) = vx;
      } else {
        *(u32x4*)(base + oQK) = *(const u32x4*)(lds + Tl * SP + hl * 48 + c * 16);
        if (t8 < 128u) {
          const unsigned xv = *(const unsigned*)(lds + t8 * SP + hl * 48 + 32);
          if (which == 0) *(unsigned*)(ws + WS_QX + (size_t)h * 8192 + T2 * 4u) = xv;
          else *(unsigned*)(base + oKX) = xv;
        }
      }
    }
  }
  if (tid >= 256 && tid - 256 < 162) {
    u32x4 v;
#pragma unroll
    for (int j = 0; j < 4; ++j) v[j] = (unsigned)wog[2 * j] | ((unsigned)wog[2 * j + 1] << 16);
    *(u32x4*)(ws + WS_WOF + (size_t)(b * 162 + tid - 256) * 16) = v;
  }
  STAMP(0, 4);
  asm volatile("s_waitcnt vmcnt(0)" ::: "memory");
#undef Q_ISSUE
#undef Q_TILE
#undef Q_WAIT
#undef Q_READ
#undef Q_MMA
#undef Q_ILV
}

#define NW2 7
#define HTHR 2
#define STR2_(x) #x
#define STR_(x) STR2_(x)
#define EX2(v) __builtin_amdgcn_exp2f(v)
__global__ __launch_bounds__(512, 1) void k2_attn(char* __restrict__ ws, const u16* __restrict__ w1, const u16* __restrict__ w2) {
  __shared__ __attribute__((aligned(16))) char lds[KVHEAD + 1024];
  const int tid = threadIdx.x, lane = tid & 63;
  const int w = __builtin_amdgcn_readfirstlane(tid >> 6);
  const int b = blockIdx.x, xg = b & 7;
  const int h = xg * 4 + ((b >> 3) & 3), qt = b >> 5;
  const int qp = w & 3, half = w >> 2;
  const unsigned lds0 = (unsigned)(uintptr_t)lds;
  const int r = lane & 31, hh = lane >> 5;
  const char* kvg = ws + WS_KV + (size_t)h * KVHEAD;
  const unsigned l16 = (unsigned)lane * 16u;
  STAMP(1, 0);
  u32x4 qmA, qmB; unsigned qxA, qxB;
  {
    const char* qmp = ws + WS_QM + (size_t)h * 65536 + (size_t)(qt * 8 + qp * 2) * 1024 + lane * 16;
    const char* qxp = ws + WS_QX + (size_t)h * 8192 + (size_t)(qt * 256 + qp * 64 + r) * 4;
    asm volatile("global_load_dwordx4 %0, %1, off" : "=v"(qmA) : "v"(qmp) : "memory");
    asm volatile("global_load_dwordx4 %0, %1, off offset:1024" : "=v"(qmB) : "v"(qmp) : "memory");
    asm volatile("global_load_dword %0, %1, off" : "=v"(qxA) : "v"(qxp) : "memory");
    asm volatile("global_load_dword %0, %1, off offset:128" : "=v"(qxB) : "v"(qxp) : "memory");
  }
#pragma unroll
  for (int i = 0; i < 6; ++i) {
    const int s = i * 8 + w;
    const int p = (s < 10) ? s : (s < 20) ? 32 + s - 10 : (s < 30) ? 64 + s - 20 : (s < 40) ? 104 + s - 30 : 144 + s - 40;
    glds16(kvg + (size_t)p * 1024, l16, lds0 + p * 1024);
  }
#define STAGEB(i) do { const int s_ = (i) * 8 + w; \
    const int p_ = (s_ < 22) ? 10 + s_ : (s_ < 44) ? 42 + s_ - 22 : (s_ < 74) ? 74 + s_ - 44 : 114 + s_ - 74; \
    glds16(kvg + (size_t)p_ * 1024, l16, lds0 + p_ * 1024); } while (0)
  const lds_cptr vp = (lds_cptr)lds + (65536u + (unsigned)half * 64u * VREC + hh * 320 + (r < 19 ? r : 19) * 16);
  const lds_cptr kp = (lds_cptr)lds + (half * 32768 + hh * 512 + r * 16);
  const lds_cptr xp = (lds_cptr)lds + (KXOFF + half * 4096 + r * 16);

  const char* pfb = (const char*)w1 + (size_t)(b * 8 + w) * 1024;
  asm volatile("s_waitcnt vmcnt(0)\n\ts_barrier" : "+v"(qmA), "+v"(qmB), "+v"(qxA), "+v"(qxB) :: "memory");
  STAMP(1, 1);
  const bf16x8 qA = __builtin_bit_cast(bf16x8, qmA), qB = __builtin_bit_cast(bf16x8, qmB);
  bf16x8 eA[4], eB[4];
  {
    const unsigned a = hh == 0 ? qxA : 0u, bq = hh == 0 ? qxB : 0u;
    eA[0] = __builtin_bit_cast(bf16x8, (u32x4){a, 0u, 0u, 0u}); eB[0] = __builtin_bit_cast(bf16x8, (u32x4){bq, 0u, 0u, 0u});
    eA[1] = __builtin_bit_cast(bf16x8, (u32x4){0u, a, 0u, 0u}); eB[1] = __builtin_bit_cast(bf16x8, (u32x4){0u, bq, 0u, 0u});
    eA[2] = __builtin_bit_cast(bf16x8, (u32x4){0u, 0u, a, 0u}); eB[2] = __builtin_bit_cast(bf16x8, (u32x4){0u, 0u, bq, 0u});
    eA[3] = __builtin_bit_cast(bf16x8, (u32x4){0u, 0u, 0u, a}); eB[3] = __builtin_bit_cast(bf16x8, (u32x4){0u, 0u, 0u, bq});
  }
  const f32x16 zero16 = {};
  f32x16 oA = {}, oB = {}, sA, sB;
  u32x4 pAl = {0u, 0u, 0u, 0u}, pAh = {0u, 0u, 0u, 0u}, pBl = {0u, 0u, 0u, 0u}, pBh = {0u, 0u, 0u, 0u};
  bf16x8 kf, kx, kxn, v0, v1 = {};
  kf = ldsr128(kp); kx = ldsr128(xp);
  sA = MFMA32(kf, qA, zero16); sA = MFMA32(kx, eA[0], sA);
  float e0 = 0.f, e1 = 0.f, e2 = 0.f, e3 = 0.f, f0, f1, f2, f3;
  SB0();
#define EX4(a, b_, c_, d_, S, i0) do { a = EX2(S[i0]); b_ = EX2(S[i0 + 1]); c_ = EX2(S[i0 + 2]); d_ = EX2(S[i0 + 3]); } while (0)
#define CV2(P, j0, a, b_, c_, d_) do { P[j0] = cvtpk(a, b_); P[j0 + 1] = cvtpk(c_, d_); } while (0)
#define PF8(x) __builtin_bit_cast(bf16x8, x)
#define PIN(x) asm volatile("" : "+v"(x))
#define PINE() asm volatile("" : "+v"(e0), "+v"(e1), "+v"(e2), "+v"(e3))
#define PINF() asm volatile("" : "+v"(f0), "+v"(f1), "+v"(f2), "+v"(f3))
#define EXA(a, b_, S, i0) do { a = EX2(S[i0]); b_ = EX2(S[i0 + 1]); asm volatile("" : "+v"(a), "+v"(b_)); SB0(); } while (0)
#define EXB(c_, d_, S, i0) do { c_ = EX2(S[i0 + 2]); d_ = EX2(S[i0 + 3]); } while (0)
#define TILE(t) do { \
    constexpr int ro_ = (t) * 2 * (int)VREC; \
      sB = MFMA32(kf, qB, zero16); PIN(sB); SB0(); v0 = ldsr128(vq + ro_); kf = ldsr128(kq + ((t) + 1) * 1024); \
           if ((t) == 3) { kxn = ldsr128(xq + 512); } \
           EX4(f0, f1, f2, f3, sA, 0); CV2(pBh, 2, e0, e1, e2, e3); PINF(); PIN(pBh); SB0(); \
      EXA(e0, e1, sA, 4); oB = MFMA32(v1, PF8(pBh), oB); PIN(oB); SB0(); v1 = ldsr128(vq + ro_ + VREC); \
           EXB(e2, e3, sA, 4); CV2(pAl, 0, f0, f1, f2, f3); PINE(); PIN(pAl); SB0(); \
      EXA(f0, f1, sA, 8); sB = MFMA32(kx, eB[t], sB); PIN(sB); SB0(); \
           EXB(f2, f3, sA, 8); CV2(pAl, 2, e0, e1, e2, e3); PINF(); PIN(pAl); SB0(); \
      EXA(e0, e1, sA, 12); __builtin_amdgcn_s_waitcnt(0xC07F); SB0(); oA = MFMA32(v0, PF8(pAl), oA); PIN(oA); SB0(); \
           EXB(e2, e3, sA, 12); CV2(pAh, 0, f0, f1, f2, f3); PINE(); PIN(pAh); SB0(); \
      EXA(f0, f1, sB, 0); sA = MFMA32(kf, qA, zero16); PIN(sA); SB0(); EXB(f2, f3, sB, 0); CV2(pAh, 2, e0, e1, e2, e3); PINF(); PIN(pAh); SB0(); \
      EXA(e0, e1, sB, 4); oA = MFMA32(v1, PF8(pAh), oA); PIN(oA); SB0(); EXB(e2, e3, sB, 4); CV2(pBl, 0, f0, f1, f2, f3); PINE(); PIN(pBl); SB0(); \
      EXA(f0, f1, sB, 8); sA = MFMA32((t) == 3 ? kxn : kx, eA[((t) + 1) & 3], sA); PIN(sA); SB0(); EXB(f2, f3, sB, 8); CV2(pBl, 2, e0, e1, e2, e3); PINF(); PIN(pBl); SB0(); \
      EXA(e0, e1, sB, 12); oB = MFMA32(v0, PF8(pBl), oB); PIN(oB); SB0(); EXB(e2, e3, sB, 12); CV2(pBh, 0, f0, f1, f2, f3); PINE(); PIN(pBh); SB0(); \
  } while (0)
#define SIDE(a) do { STAGEB(it * 8 + (a)); if (it * 8 + (a) + 1 < 13) STAGEB(it * 8 + (a) + 1); SB0(); } while (0)
#define PFETCH() do { glds16(pfb, l16, lds0 + KVHEAD); pfb += 2048 * 1024; if (++pfn == 9) pfb = (const char*)w2 + (size_t)(b * 8 + w) * 1024; SB0(); } while (0)
  int pfn = 0;
  lds_cptr vq = vp, kq = kp, xq = xp;
  const auto slabr = __builtin_amdgcn_make_buffer_rsrc(ws + WS_SLAB + (size_t)b * 40960, 0, 40960, 0x00020000);
  __builtin_amdgcn_s_setprio(3);
#pragma unroll 1
  for (int it = 0; it < 2; ++it) {
    SIDE(0); TILE(0);
    SIDE(2); TILE(1);
    SIDE(4); TILE(2);
    if (it == 0) SIDE(6);
    TILE(3);
    kx = kxn;
    vq += 8 * VREC; kq += 4096; xq += 512;
  }
  asm volatile("s_waitcnt vmcnt(0)\n\ts_barrier" ::: "memory");
  STAMP(1, 2);
#pragma unroll 1
  for (int it = 2; it < 8; ++it) {
    if (it == 4) __builtin_amdgcn_s_setprio(2);
    if (it == 6) __builtin_amdgcn_s_setprio(1);
    if (it == 7) __builtin_amdgcn_s_setprio(0);
    SB0();
    PFETCH(); TILE(0);
    if (it >= 11 - NW2) { PFETCH(); }
    TILE(1);
    PFETCH();
    TILE(2);
    if (it < 7) {
      __builtin_amdgcn_raw_buffer_store_b128((u32x4){~0u, ~0u, ~0u, ~0u}, slabr, (w * 5 + it - 2) * 1024 + lane * 16, 0, 16);
      SB0();
    }
    TILE(3);
    kx = kxn;
    vq += 8 * VREC; kq += 4096; xq += 512;
  }
  CV2(pBh, 2, e0, e1, e2, e3);
  oB = MFMA32(v1, PF8(pBh), oB);
  STAMP(1, 3);

  float mA = 0.f, mB = 0.f;
  {
    bool bad = false;
    if (hh == 0) {
      bad = !(oA[10] >= 8.271806125530277e-25f && oA[10] <= 1.2676506002282294e30f) ||
            !(oB[10] >= 8.271806125530277e-25f && oB[10] <= 1.2676506002282294e30f);
    }
#pragma unroll
    for (int i = 0; i < 10; ++i) bad |= !(__builtin_fabsf(oA[i]) <= 3.0e38f) || !(__builtin_fabsf(oB[i]) <= 3.0e38f);
    if (__any(bad)) {
#pragma unroll 1
      for (int qq = 0; qq < 2; ++qq) {
        const bf16x8 qf = qq ? qB : qA;
        f32x16 oacc = zero16;
        float m = -INFINITY;
        for (int it = 0; it < 8; ++it) {
          const bf16x8 kx_ = ldsr128(xp + it * 512);
#pragma unroll
          for (int t = 0; t < 4; ++t) {
            const bf16x8 kf_ = ldsr128(kp + (it * 4 + t) * 1024);
            f32x16 s_ = MFMA32(kf_, qf, zero16);
            s_ = MFMA32(kx_, qq ? eB[t] : eA[t], s_);
            float tm = s_[0];
#pragma unroll
            for (int i = 1; i < 16; ++i) tm = fmaxf(tm, s_[i]);
            tm = fmaxf(tm, __shfl_xor(tm, 32));
            const float mn = fmaxf(m, tm);
            const float sc = EX2(m - mn);
            m = mn;
#pragma unroll
            for (int i = 0; i < 16; ++i) oacc[i] *= sc;
            const int ro_ = (it * 8 + t * 2) * (int)VREC;
            const bf16x8 v0_ = ldsr128(vp + ro_), v1_ = ldsr128(vp + ro_ + VREC);
            u32x4 pa_, pb_;
#pragma unroll
            for (int i = 0; i < 4; ++i) {
              pa_[i] = cvtpk(EX2(s_[2 * i] - m), EX2(s_[2 * i + 1] - m));
              pb_[i] = cvtpk(EX2(s_[8 + 2 * i] - m), EX2(s_[8 + 2 * i + 1] - m));
            }
            oacc = MFMA32(v0_, PF8(pa_), oacc);
            oacc = MFMA32(v1_, PF8(pb_), oacc);
          }
        }
        if (qq) { oB = oacc; mB = m; } else { oA = oacc; mA = m; }
      }
    }
  }
  const unsigned hm0 = eqmask(half, 0), hm1 = eqmask(half, 1);
  BARRIER();
  {
    f32x4* mb = (f32x4*)(lds + w * 4096);
    float os[11];
#pragma unroll
    for (int i = 0; i < 11; ++i) os[i] = sel4(oA[i], oB[i], 0.f, 0.f, hm1, hm0, 0u, 0u);
    const float ms = sel4(mA, mB, 0.f, 0.f, hm1, hm0, 0u, 0u);
    mb[lane] = (f32x4){os[0], os[1], os[2], os[3]};
    mb[64 + lane] = (f32x4){os[4], os[5], os[6], os[7]};
    mb[128 + lane] = (f32x4){os[8], os[9], os[10], ms};
  }
  BARRIER();
  {
    const f32x4* mb = (const f32x4*)(lds + (w ^ 4) * 4096);
    float ok[11];
#pragma unroll
    for (int i = 0; i < 11; ++i) ok[i] = sel4(oA[i], oB[i], 0.f, 0.f, hm0, hm1, 0u, 0u);
    const float mk = sel4(mA, mB, 0.f, 0.f, hm0, hm1, 0u, 0u);
    const f32x4 a0 = mb[lane], a1 = mb[64 + lane], a2 = mb[128 + lane];
    const float M = fmaxf(mk, a2[3]);
    const float s0 = EX2(mk - M), s1 = EX2(a2[3] - M);
    const float lsum = __shfl(ok[10] * s0 + a2[2] * s1, r);
    const float inv = 1.0f / lsum;
    const float c0 = s0 * inv, c1 = s1 * inv;
    u32x4 ov;
    ov[0] = cvtpk(ok[0] * c0 + a0[0] * c1, ok[1] * c0 + a0[1] * c1);
    ov[1] = cvtpk(ok[2] * c0 + a0[2] * c1, ok[3] * c0 + a0[3] * c1);
    ov[2] = cvtpk(ok[4] * c0 + a1[0] * c1, ok[5] * c0 + a1[1] * c1);
    ov[3] = cvtpk(ok[6] * c0 + a1[2] * c1, ok[7] * c0 + a1[3] * c1);
    const int T = qt * 256 + qp * 64 + half * 32 + r;
    char* ofb = ws + WS_OF + (size_t)(T >> 4) * 18432;
    *(u32x4*)(ofb + (h >> 1) * 1024 + (((h & 1) * 2 + hh) * 16 + (T & 15)) * 16) = ov;
    if (hh == 0)
      *(unsigned*)(ofb + (16 + (h >> 4)) * 1024 + (((h & 15) >> 2) * 16 + (T & 15)) * 16 + (h & 3) * 4) =
          cvtpk(ok[8] * c0 + a2[0] * c1, ok[9] * c0 + a2[1] * c1);
  }
  STAMP(1, 4);
  asm volatile("s_waitcnt vmcnt(0)" ::: "memory");
#undef TILE
#undef SIDE
#undef PFETCH
#undef STAGEB
#undef EX4
#undef CV2
}

__device__ __forceinline__ float row16_sum(float v) {
  v += __builtin_bit_cast(float, __builtin_amdgcn_update_dpp(0, __builtin_bit_cast(int, v), 0x128, 0xf, 0xf, false));
  v += __builtin_bit_cast(float, __builtin_amdgcn_update_dpp(0, __builtin_bit_cast(int, v), 0x124, 0xf, 0xf, false));
  v += __builtin_bit_cast(float, __builtin_amdgcn_update_dpp(0, __builtin_bit_cast(int, v), 0x122, 0xf, 0xf, false));
  v += __builtin_bit_cast(float, __builtin_amdgcn_update_dpp(0, __builtin_bit_cast(int, v), 0x121, 0xf, 0xf, false));
  return v;
}
__global__ __launch_bounds__(576, 1) void k3_proj(const u16* __restrict__ x, char* __restrict__ ws) {
  __shared__ __attribute__((aligned(16))) char lds[36864 + 9 * 256 + 256];
  const int tid = threadIdx.x, lane = tid & 63;
  const int w = __builtin_amdgcn_readfirstlane(tid >> 6);
  const int b = blockIdx.x, xg = b & 7, o = b >> 3;
  const int tt32 = xg * 8 + (o >> 2), ct = o & 3;
  const int g = lane >> 4, c16 = lane & 15;
  const int nt = ct * 9 + w;
  const unsigned lds0 = (unsigned)(uintptr_t)lds;
  STAMP(2, 0);
#pragma unroll
  for (int i = 0; i < 4; ++i)
    glds16(ws + WS_OF + (size_t)(tt32 * 2) * 18432 + (size_t)(i * 9 + w) * 1024, (unsigned)lane * 16u, lds0 + (unsigned)(i * 9 + w) * 1024u);
  const char* wfp = ws + WS_WOF + (size_t)nt * 18432 + lane * 16;
  bf16x8 bfr[18];
#pragma unroll
  for (int ks = 0; ks < 18; ++ks) bfr[ks] = *(const bf16x8*)(wfp + ks * 1024);
  u16 xr[2][4];
#pragma unroll
  for (int t = 0; t < 2; ++t)
#pragma unroll
    for (int j = 0; j < 4; ++j) xr[t][j] = x[(size_t)(tt32 * 32 + t * 16 + g * 4 + j) * 576 + nt * 16 + c16];
  asm volatile("s_waitcnt vmcnt(26)\n\ts_barrier" ::: "memory");
  f32x4 acc[2] = {(f32x4){0, 0, 0, 0}, (f32x4){0, 0, 0, 0}};
#pragma unroll
  for (int ks = 0; ks < 18; ++ks) {
    acc[0] = MFMA16(ldsr128((lds_cptr)lds + ks * 1024 + lane * 16), bfr[ks], acc[0]);
    acc[1] = MFMA16(ldsr128((lds_cptr)lds + 18432 + ks * 1024 + lane * 16), bfr[ks], acc[1]);
  }
  float* hp = (float*)(ws + WS_H);
  float* pst = (float*)(lds + 36864);
  float* fin = (float*)(lds + 36864 + 9 * 256);
#pragma unroll
  for (int t = 0; t < 2; ++t)
#pragma unroll
    for (int j = 0; j < 4; ++j) {
      const float hv = acc[t][j] + bf2f(xr[t][j]);
      acc[t][j] = hv;
      hp[(size_t)((tt32 * 2 + t) * 36 + nt) * 256 + ((c16 >> 2) * 16 + g * 4 + j) * 4 + (c16 & 3)] = hv;
      const float sm = row16_sum(hv);
      const float d = hv - sm * (1.f / 16.f);
      const float m2 = row16_sum(d * d);
      if (c16 == 0) { pst[(w * 32 + t * 16 + g * 4 + j) * 2] = sm; pst[(w * 32 + t * 16 + g * 4 + j) * 2 + 1] = m2; }
    }
  STAMP(2, 1);
  BARRIER();
  if (w == 0) {
    const int tok = lane & 31, st = lane >> 5;
    float sw[9], S = 0.f, M2 = 0.f;
#pragma unroll
    for (int i = 0; i < 9; ++i) { sw[i] = pst[(i * 32 + tok) * 2]; M2 += pst[(i * 32 + tok) * 2 + 1]; S += sw[i]; }
    const float mub = S * (1.f / 144.f);
#pragma unroll
    for (int i = 0; i < 9; ++i) { const float d = sw[i] * (1.f / 16.f) - mub; M2 += 16.f * d * d; }
    gu64* gr = (gu64*)(ws + WS_GRAN + (size_t)tt32 * 2048);
    __hip_atomic_store(gr + ct * 64 + lane, (1ull << 32) | (unsigned long long)__float_as_uint(st ? M2 : S), RLX_AGENT);
    float pv[4];
    for (unsigned spins = 0;; ++spins) {
      bool ok = true;
#pragma unroll
      for (int i = 0; i < 4; ++i) {
        const unsigned long long v = __hip_atomic_load(gr + i * 64 + lane, RLX_AGENT);
        pv[i] = __uint_as_float((unsigned)v);
        ok &= (v >> 32) == 1ull;
      }
      if (__all(ok) || spins > (1u << 22)) break;
      __builtin_amdgcn_s_sleep(1);
    }
    const float tot = (pv[0] + pv[1]) + (pv[2] + pv[3]);
    const float mu = tot * (1.f / 576.f);
    float extra = 0.f;
#pragma unroll
    for (int i = 0; i < 4; ++i) { const float d = pv[i] * (1.f / 144.f) - mu; extra += 144.f * d * d; }
    const float other = __shfl_xor(tot, 32);
    const float var = (other + extra) * (1.f / 576.f);
    const float r1 = rsqrtf(var + LN_EPS);
    const float r2 = rsqrtf(var * r1 * r1 + LN_EPS);
    if (st == 0) { fin[tok * 2] = mu; fin[tok * 2 + 1] = r1 * r2; }
  }
  BARRIER();
  STAMP(2, 2);
  char* hnp = ws + WS_HN;
#pragma unroll
  for (int t = 0; t < 2; ++t) {
    const f32x4 f0 = *(const f32x4*)(fin + (t * 16 + g * 4) * 2), f1 = *(const f32x4*)(fin + (t * 16 + g * 4) * 2 + 4);
    u32x2 v;
    v[0] = cvtpk((acc[t][0] - f0[0]) * f0[1], (acc[t][1] - f0[2]) * f0[3]);
    v[1] = cvtpk((acc[t][2] - f1[0]) * f1[1], (acc[t][3] - f1[2]) * f1[3]);
    *(u32x2*)(hnp + ((size_t)(nt * 64 + tt32) * 64 + (2 * t + (g >> 1)) * 16 + c16) * 16 + (g & 1) * 8) = v;
  }
  STAMP(2, 3);
}

__device__ __forceinline__ float gelu_tanh(float v) {
  const float z = v * (0.7978845608028654f + 0.035677408136300125f * v * v);
  const float e = __builtin_amdgcn_exp2f(z * -2.8853900817779268f);
  return v * __builtin_amdgcn_rcpf(1.0f + e);
}
constexpr int G1_SB = 28672;
__global__ __launch_bounds__(512, 1) void k4_gemm1(const u16* __restrict__ w1, const u16* __restrict__ b1, const u16* __restrict__ w2,
                                                    char* __restrict__ ws) {
  __shared__ __attribute__((aligned(16))) char lds[4 * G1_SB];
  const int tid = threadIdx.x, lane = tid & 63;
  const int w8 = __builtin_amdgcn_readfirstlane(tid >> 6), w = w8 & 3;
  const unsigned lds0 = (unsigned)(uintptr_t)lds;
  if (blockIdx.x >= 216) {
    const char* p = (const char*)w2 + (size_t)(NW2 * 2048 + (blockIdx.x - 216) * 8 + w8) * 1024;
#pragma unroll 1
    for (int j = NW2 * 2048 + (blockIdx.x - 216) * 8 + w8; j < 18432; j += 320) {
      glds16(p, (unsigned)lane * 16u, lds0 + (unsigned)w8 * 1024u);
      p += 320 * 1024;
      asm volatile("s_waitcnt vmcnt(" STR_(HTHR) ")" ::: "memory");
    }
    asm volatile("s_waitcnt vmcnt(0)" ::: "memory");
    return;
  }
  const int id = (blockIdx.x & 7) * 27 + (blockIdx.x >> 3);
  const int pn = id / 6, pm = id - pn * 6;
  STAMP(3, 0);
  if (w8 >= 4) {
    const char* hn = ws + WS_HN + (size_t)(pm * 6) * 65536;
    unsigned aoff[3];
#pragma unroll
    for (int i = 0; i < 3; ++i) { const int a = w + 4 * i; aoff[i] = (unsigned)((a >> 1) * 65536 + (a & 1) * 1024 + lane * 16); }
    const char* wb = (const char*)w1 + (size_t)((pn >> 2) * 8 + (pn & 3)) * 128;
    unsigned boff[4];
#pragma unroll
    for (int i = 0; i < 4; ++i) {
      const int row = (4 * w + i) * 4 + (lane >> 4), cp = lane & 15;
      const int f = (row & 3) | (((row >> 3) & 1) << 2);
      const int ch = (((cp >> 1) ^ f) << 1) | (cp & 1);
      boff[i] = (unsigned)(row * 9216 + (ch & 7) * 16 + (ch >> 3) * 512);
    }
#define G1_ISSUE(t) do { const unsigned sl_ = lds0 + (unsigned)((t) & 3) * G1_SB; \
    const char* ab_ = hn + (size_t)(t) * 2048; const char* bb_ = wb + (size_t)(t) * (64 * 9216); \
    _Pragma("unroll") for (int i = 0; i < 4; ++i) glds16(bb_, boff[i], sl_ + 12288u + (unsigned)(4 * w + i) * 1024u); \
    _Pragma("unroll") for (int i = 0; i < 3; ++i) glds16(ab_, aoff[i], sl_ + (unsigned)(w + 4 * i) * 1024u); } while (0)
    G1_ISSUE(0); G1_ISSUE(1); G1_ISSUE(2);
#pragma unroll 1
    for (int t = 0; t < 29; ++t) {
      asm volatile("s_waitcnt vmcnt(14)\n\ts_barrier" ::: "memory");
      G1_ISSUE(t + 3);
    }
    asm volatile("s_waitcnt vmcnt(14)\n\ts_barrier" ::: "memory");
    asm volatile("s_waitcnt vmcnt(7)\n\ts_barrier" ::: "memory");
    asm volatile("s_waitcnt vmcnt(0)\n\ts_barrier" ::: "memory");
    return;
  }
  const int wr = w >> 1, wc = w & 1;
  const int g = lane >> 4, c16 = lane & 15, q4 = (lane & 15) >> 2, p4 = lane & 3;
  const int fl = q4 | ((g & 1) << 2);
  lds_cptr bp0[4], bp1[4];
#pragma unroll
  for (int n = 0; n < 4; ++n) {
    bp0[n] = (lds_cptr)lds + (12288 + (8 * g + q4) * 256 + (((wc * 4 + n) ^ fl) * 32) + 8 * p4);
    bp1[n] = bp0[n] + 2 * G1_SB;
    asm volatile("" : "+v"(bp1[n]));
  }
  const lds_cptr ap0 = (lds_cptr)lds + (wr * 6144 + lane * 16);
  lds_cptr ap1 = ap0 + 2 * G1_SB;
  asm volatile("" : "+v"(ap1));
  f32x4 acc[4][3];
#pragma unroll
  for (int n = 0; n < 4; ++n)
#pragma unroll
    for (int m = 0; m < 3; ++m) acc[n][m] = (f32x4){0, 0, 0, 0};
  bf16x8 aX[3], bX[4], aY[3], bY[4];
#define G1_READ(A_, B_, sl, ks) do { const lds_cptr a_ = ((sl) < 2 ? ap0 : ap1) + ((sl) & 1) * G1_SB + (ks) * 1024; \
    _Pragma("unroll") for (int m = 0; m < 3; ++m) A_[m] = ldsr128(a_ + m * 2048); \
    _Pragma("unroll") for (int n = 0; n < 4; ++n) { const lds_cptr b_ = ((sl) < 2 ? bp0[n] : bp1[n]) + ((sl) & 1) * G1_SB + (ks) * 8192; \
      B_[n] = cat8(trread(b_), trread(b_ + 1024)); } } while (0)
#define G1_MMA(A_, B_) do { _Pragma("unroll") for (int n = 0; n < 4; ++n) _Pragma("unroll") for (int m = 0; m < 3; ++m) \
    acc[n][m] = MFMA16(B_[n], A_[m], acc[n][m]); } while (0)
#define G1_ILV() do { _Pragma("unroll") for (int i_ = 0; i_ < 6; ++i_) { SGB(0x8, 1); SGB(0x100, 2); } SGB(0x8, 6); } while (0)
#define G1_STAGE(sl) do { \
    G1_READ(aY, bY, sl, 1); G1_MMA(aX, bX); G1_ILV(); SB0(); \
    BARRIER(); SB0(); \
    G1_READ(aX, bX, ((sl) + 1) & 3, 0); G1_MMA(aY, bY); G1_ILV(); SB0(); } while (0)
  asm volatile("" :: "s"(b1), "s"(ws));
  BARRIER();
  STAMP(3, 1);
  __builtin_amdgcn_s_waitcnt(0xC07F);
  G1_READ(aX, bX, 0, 0);
  SB0();
#pragma unroll 1
  for (int t = 0; t < 8; ++t) { G1_STAGE(0); G1_STAGE(1); G1_STAGE(2); G1_STAGE(3); }
  STAMP(3, 3);
  char* up = ws + WS_U;
#pragma unroll
  for (int n = 0; n < 4; ++n) {
    const int ft = ((pn >> 2) * 8 + (pn & 3) + 4 * wc) * 4 + n;
    const u32x2 bv = *(const u32x2*)(b1 + ft * 16 + g * 4);
    const float bb0 = __uint_as_float(bv[0] << 16), bb1 = __uint_as_float(bv[0] & 0xffff0000u);
    const float bb2 = __uint_as_float(bv[1] << 16), bb3 = __uint_as_float(bv[1] & 0xffff0000u);
#pragma unroll
    for (int m = 0; m < 3; ++m) {
      const int dt = pm * 6 + wr * 3 + m;
      u32x2 v;
      v[0] = cvtpk(gelu_tanh(acc[n][m][0] + bb0), gelu_tanh(acc[n][m][1] + bb1));
      v[1] = cvtpk(gelu_tanh(acc[n][m][2] + bb2), gelu_tanh(acc[n][m][3] + bb3));
      *(u32x2*)(up + ((size_t)(dt * 144 + (ft >> 1)) * 64 + ((ft & 1) * 2 + (g >> 1)) * 16 + c16) * 16 + (g & 1) * 8) = v;
    }
  }
  STAMP(3, 4);
#undef G1_ISSUE
#undef G1_READ
#undef G1_MMA
#undef G1_ILV
#undef G1_STAGE
}

constexpr int G2_ST = 4, G2_SB = 36864;
__global__ __launch_bounds__(512, 1) void k5_gemm2(const u16* __restrict__ w2, const u16* __restrict__ b2, char* __restrict__ ws,
                                                    u16* __restrict__ out) {
  __shared__ __attribute__((aligned(16))) char lds[G2_ST * G2_SB];
  const int tid = threadIdx.x, lane = tid & 63;
  const int w8 = __builtin_amdgcn_readfirstlane(tid >> 6), w = w8 & 3;
  const int xg = blockIdx.x & 7, o = blockIdx.x >> 3;
  const int kq = xg & 3, pm = o & 3, pn = (xg >> 2) * 8 + (o >> 2);
  const int tile = pn * 4 + pm;
  const unsigned lds0 = (unsigned)(uintptr_t)lds;
  STAMP(4, 0);
  if (w8 >= 4) {
    const char* ub = ws + WS_U + (size_t)(pm * 9) * 147456 + (size_t)kq * 36864;
    unsigned aoff[5];
#pragma unroll
    for (int i = 0; i < 5; ++i) { int a = w + 4 * i; if (a >= 18) a -= 4; aoff[i] = (unsigned)((a >> 1) * 147456 + (a & 1) * 1024 + lane * 16); }
    const char* wb = (const char*)w2 + (size_t)kq * 1152 * 4096 + (size_t)((pn >> 2) * 8 + (pn & 3)) * 128;
    unsigned boff[4];
#pragma unroll
    for (int i = 0; i < 4; ++i) {
      const int row = (4 * w + i) * 4 + (lane >> 4), cp = lane & 15;
      const int f = (row & 3) | (((row >> 3) & 1) << 2);
      const int ch = (((cp >> 1) ^ f) << 1) | (cp & 1);
      boff[i] = (unsigned)(row * 4096 + (ch & 7) * 16 + (ch >> 3) * 512);
    }
#define G2_ISSUE(NA, t, slot) do { const unsigned sl_ = lds0 + (unsigned)(slot) * G2_SB; \
    const char* ab_ = ub + (size_t)(t) * 2048; const char* bb_ = wb + (size_t)(t) * (64 * 4096); \
    _Pragma("unroll") for (int i = 0; i < 4; ++i) glds16(bb_, boff[i], sl_ + 20480u + (unsigned)(4 * w + i) * 1024u); \
    _Pragma("unroll") for (int i = 0; i < NA; ++i) glds16(ab_, aoff[i], sl_ + (unsigned)(w + 4 * i) * 1024u); } while (0)
#define G2_LOADER(NA, W2_, W1_) do { \
    _Pragma("unroll") for (int t = 0; t < G2_ST - 1; ++t) G2_ISSUE(NA, t, t); \
    int islot = G2_ST - 1; \
    for (int t = 0; t < 18 - (G2_ST - 1); ++t) { \
      asm volatile("s_waitcnt vmcnt(" #W2_ ")\n\ts_barrier" ::: "memory"); \
      G2_ISSUE(NA, t + G2_ST - 1, islot); \
      islot = (islot + 1 == G2_ST) ? 0 : islot + 1; } \
    asm volatile("s_waitcnt vmcnt(" #W2_ ")\n\ts_barrier" ::: "memory"); \
    _Pragma("unroll") for (int i4 = 0; i4 < 5; ++i4) {         \
      const int tau_ = ((i4 == 4 && kq >= 2) ? 3 : i4) * 4 + kq; \
      const int st_ = ((pn >> 2) * 8 + (pn & 3) + 4 * (w >> 1)) * 4 + (w & 1) * 2 + (tau_ & 1); \
      glds16(ws + WS_H + (size_t)(st_ * 36 + pm * 9 + (tau_ >> 1)) * 1024, (unsigned)lane * 16u, lds0 + 2u * G2_SB + (unsigned)(w * 5 + i4) * 1024u); } \
    asm volatile("s_waitcnt vmcnt(" #W1_ ")\n\ts_barrier" ::: "memory"); \
    asm volatile("s_waitcnt vmcnt(0)\n\ts_barrier" ::: "memory"); } while (0)
    if (w < 2) G2_LOADER(5, 18, 14); else G2_LOADER(4, 16, 13);
    return;
  }
  const int g = lane >> 4, c16 = lane & 15, q4 = (lane & 15) >> 2, p4 = lane & 3;
  const int fl = q4 | ((g & 1) << 2);
  unsigned brd[2];
#pragma unroll
  for (int n = 0; n < 2; ++n) brd[n] = (unsigned)(20480 + (8 * g + q4) * 256 + (((w * 2 + n) ^ fl) * 32) + 8 * p4);
  const unsigned ard = (unsigned)(lane * 16);
  u16 bb[5]; unsigned oo[5];
#pragma unroll
  for (int i4 = 0; i4 < 5; ++i4) {
    const int ie = (i4 == 4 && kq >= 2) ? 3 : i4;
    const int tau = ie * 4 + kq, m = tau >> 1, n = tau & 1;
    const int sp_ = ((pn >> 2) * 8 + (pn & 3) + 4 * (w >> 1)) * 64 + ((w & 1) * 2 + n) * 16 + c16, d0 = pm * 144 + m * 16 + g * 4;
    oo[i4] = (unsigned)(sp_ * 576 + d0);
    bb[i4] = b2[sp_];
  }
  f32x4 acc[18];
#pragma unroll
  for (int i = 0; i < 18; ++i) acc[i] = (f32x4){0, 0, 0, 0};
  int slot = 0;
  bf16x8 aX[9], bX[2], aY[9], bY[2];
#define G2_READ(A_, B_, ks) do { const lds_cptr sp_ = (lds_cptr)lds + slot * G2_SB; \
    _Pragma("unroll") for (int n = 0; n < 2; ++n) B_[n] = cat8(trread(sp_ + brd[n] + (ks) * 8192), trread(sp_ + brd[n] + (ks) * 8192 + 1024)); \
    _Pragma("unroll") for (int m = 0; m < 9; ++m) A_[m] = ldsr128(sp_ + ard + m * 2048 + (ks) * 1024); } while (0)
#define G2_MMA(A_, B_) do { _Pragma("unroll") for (int m = 0; m < 9; ++m) { \
    acc[m * 2] = MFMA16(A_[m], B_[0], acc[m * 2]); acc[m * 2 + 1] = MFMA16(A_[m], B_[1], acc[m * 2 + 1]); } } while (0)
#define G2_ILV() do { _Pragma("unroll") for (int i_ = 0; i_ < 13; ++i_) { SGB(0x8, 1); SGB(0x100, 1); } SGB(0x8, 5); } while (0)
  asm volatile("" :: "s"(b2), "s"(ws), "s"(out));
  BARRIER();
  STAMP(4, 1);
  __builtin_amdgcn_s_waitcnt(0xC07F);
  G2_READ(aX, bX, 0);
  SB0();
  for (int t = 0; t < 17; ++t) {
    G2_READ(aY, bY, 1); G2_MMA(aX, bX); G2_ILV(); SB0();
    BARRIER();
    slot = (slot + 1 == G2_ST) ? 0 : slot + 1;
    SB0();
    G2_READ(aX, bX, 0); G2_MMA(aY, bY); G2_ILV(); SB0();
  }
  G2_READ(aY, bY, 1); G2_MMA(aX, bX); G2_ILV(); SB0();
  G2_MMA(aY, bY);
  STAMP(4, 3);
  const auto rsrc = __builtin_amdgcn_make_buffer_rsrc(ws + WS_SLAB + (size_t)tile * SLABT, 0, SLABT, 0x00020000);
#define PKW(i, e) __builtin_elementwise_min(cvtpk(acc[i][2 * (e)], acc[i][2 * (e) + 1]), 0xFFFFFFFEu)
#pragma unroll
  for (int dst = 0; dst < 4; ++dst) {
    if (dst != kq) {
      const int sb = (dst * 4 + kq) * 10240 + w * 2560;
      __builtin_amdgcn_raw_buffer_store_b128((u32x4){PKW(dst, 0), PKW(dst, 1), PKW(dst + 4, 0), PKW(dst + 4, 1)}, rsrc, sb + lane * 16, 0, 16);
      __builtin_amdgcn_raw_buffer_store_b128((u32x4){PKW(dst + 8, 0), PKW(dst + 8, 1), PKW(dst + 12, 0), PKW(dst + 12, 1)}, rsrc, sb + 1024 + lane * 16, 0, 16);
      if (dst < 2) __builtin_amdgcn_raw_buffer_store_b64((u32x2){PKW((dst + 16) % 18, 0), PKW((dst + 16) % 18, 1)}, rsrc, sb + 2048 + lane * 8, 0, 16);
    }
  }
#undef PKW
  STAMP(4, 4);
  f32x4 own[5];
  const unsigned km0 = eqmask(kq, 0), km1 = eqmask(kq, 1), km2 = eqmask(kq, 2), km3 = eqmask(kq, 3);
#pragma unroll
  for (int i4 = 0; i4 < 5; ++i4) {
#pragma unroll
    for (int j = 0; j < 4; ++j)
      own[i4][j] = sel4(acc[i4 * 4][j], acc[i4 * 4 + 1][j], acc[(i4 * 4 + 2) % 18][j], acc[(i4 * 4 + 3) % 18][j], km0, km1, km2, km3);
    own[i4] += *(const f32x4*)(lds + 2 * G2_SB + (w * 5 + i4) * 1024 + lane * 16) + bf2f(bb[i4]);
  }
  asm volatile("s_waitcnt vmcnt(0)" ::: "memory");
  u32x4 pa[3], pb[3]; u32x2 pc[3];
  const int x8 = (kq < 2) ? 2048 + lane * 8 : lane * 16;
  for (unsigned spins = 0;; ++spins) {
#pragma unroll
    for (int s = 0; s < 3; ++s) {
      const int sb = (kq * 4 + ((kq + s + 1) & 3)) * 10240 + w * 2560;
      pa[s] = __builtin_amdgcn_raw_buffer_load_b128(rsrc, sb + lane * 16, 0, 16);
      pb[s] = __builtin_amdgcn_raw_buffer_load_b128(rsrc, sb + 1024 + lane * 16, 0, 16);
      pc[s] = __builtin_amdgcn_raw_buffer_load_b64(rsrc, sb + x8, 0, 16);
    }
    unsigned mx = 0;
#pragma unroll
    for (int s = 0; s < 3; ++s) {
#pragma unroll
      for (int e = 0; e < 4; ++e) mx = max(mx, max(pa[s][e], pb[s][e]));
      mx = max(mx, max(pc[s][0], pc[s][1]));
    }
    if (__all(mx != 0xFFFFFFFFu) || spins > (1u << 20)) break;
    __builtin_amdgcn_s_sleep(2);
  }
  STAMP(4, 5);
#define UPK(d) (f32x4){__uint_as_float((d)[0] << 16), __uint_as_float((d)[0] & 0xffff0000u), __uint_as_float((d)[1] << 16), __uint_as_float((d)[1] & 0xffff0000u)}
#pragma unroll
  for (int i4 = 0; i4 < 5; ++i4) {
    f32x4 a = own[i4];
#pragma unroll
    for (int s = 0; s < 3; ++s) {
      const u32x2 d = (i4 == 0) ? (u32x2){pa[s][0], pa[s][1]} : (i4 == 1) ? (u32x2){pa[s][2], pa[s][3]} :
                      (i4 == 2) ? (u32x2){pb[s][0], pb[s][1]} : (i4 == 3) ? (u32x2){pb[s][2], pb[s][3]} : pc[s];
      a += UPK(d);
    }
    if (i4 < 4 || kq < 2) *(u32x2*)(out + oo[i4]) = (u32x2){cvtpk(a[0], a[1]), cvtpk(a[2], a[3])};
  }
#undef UPK
  STAMP(4, 6);
#undef G2_ISSUE
#undef G2_READ
#undef G2_MMA
#undef G2_LOADER
}

extern "C" void kernel_launch(void* const* d_in, const int* in_sizes, int n_in,
                              void* d_out, int out_size, void* d_ws, size_t ws_size,
                              hipStream_t stream) {
  const u16* x    = (const u16*)d_in[0];
  const u16* wqkv = (const u16*)d_in[1];
  const u16* wo   = (const u16*)d_in[2];
  const u16* w1   = (const u16*)d_in[4];
  const u16* b1   = (const u16*)d_in[5];
  const u16* w2   = (const u16*)d_in[6];
  const u16* b2   = (const u16*)d_in[7];
  char* ws = (char*)d_ws;
  k1_qkv<<<256, 512, 0, stream>>>(x, wqkv, wo, ws);
  k2_attn<<<256, 512, 0, stream>>>(ws, w1, w2);
  k3_proj<<<256, 576, 0, stream>>>(x, ws);
  k4_gemm1<<<256, 512, 0, stream>>>(w1, b1, w2, ws);
  k5_gemm2<<<256, 512, 0, stream>>>(w2, b2, ws, (u16*)d_out);
}
```

```cpp
#include <hip/hip_runtime.h>
#include <stdint.h>

typedef short    bf16x8 __attribute__((ext_vector_type(8)));
typedef short    s16x4  __attribute__((ext_vector_type(4)));
typedef float    f32x4  __attribute__((ext_vector_type(4)));
typedef float    f32x16 __attribute__((ext_vector_type(16)));
typedef unsigned u32x2  __attribute__((ext_vector_type(2)));
typedef unsigned u32x4  __attribute__((ext_vector_type(4)));
typedef __bf16   bf16x2_t __attribute__((ext_vector_type(2)));
typedef unsigned short u16;
#define LDS_AS __attribute__((address_space(3)))
#define GLB_AS __attribute__((address_space(1)))
typedef LDS_AS const char* lds_cptr;
typedef GLB_AS unsigned gu32;
typedef GLB_AS unsigned long long gu64;
#define RLX_AGENT __ATOMIC_RELAXED, __HIP_MEMORY_SCOPE_AGENT

constexpr int S_ = 2048, D_ = 576, F_ = 4608;
constexpr float LN_EPS = 1e-5f;
constexpr float QSCALE = 0.23570226039551584f * 1.4426950408889634f;

constexpr unsigned VREC    = 640;
constexpr unsigned KXOFF   = 65536 + 128 * VREC;
constexpr unsigned KVHEAD  = KXOFF + 8192;
constexpr size_t WS_QM   = 0;
constexpr size_t WS_QX   = WS_QM + 32u * 65536;
constexpr size_t WS_KV   = WS_QX + 32u * 8192;
constexpr size_t WS_OF   = WS_KV + 32u * KVHEAD;
constexpr size_t WS_WOF  = WS_OF + 128u * 18 * 1024;
constexpr size_t WS_HN   = WS_WOF + 36u * 18 * 1024;
constexpr size_t WS_H    = WS_HN + 36u * 64 * 1024;
constexpr size_t WS_U    = WS_H + 2048u * 576 * 4;
constexpr size_t WS_GRAN = WS_U + 36u * 144 * 1024;
constexpr size_t WS_SLAB = WS_GRAN + 64u * 4 * 512;
constexpr unsigned SLABT = 16u * 10240;

__device__ __forceinline__ unsigned cvtpk(float lo, float hi) {
  typedef float f32x2_t __attribute__((ext_vector_type(2)));
  return __builtin_bit_cast(unsigned, __builtin_convertvector((f32x2_t){lo, hi}, bf16x2_t));
}
__device__ __forceinline__ float bf2f(u16 v) { return __uint_as_float((unsigned)v << 16); }
__device__ __forceinline__ void glds16(const void* sbase, unsigned voff, unsigned lds_base) {
  unsigned sv;
  asm volatile("s_mov_b32 %0, m0\n\ts_mov_b32 m0, %3\n\ts_nop 0\n\tglobal_load_lds_dwordx4 %1, %2\n\ts_mov_b32 m0, %0"
               : "=&s"(sv) : "v"(voff), "s"(sbase), "s"(lds_base) : "memory");
}
__device__ __forceinline__ void touch4(const void* sbase, unsigned voff, unsigned lds_base) {
  unsigned sv;
  asm volatile("s_mov_b32 %0, m0\n\ts_mov_b32 m0, %3\n\ts_nop 0\n\tglobal_load_lds_dword %1, %2\n\ts_mov_b32 m0, %0"
               : "=&s"(sv) : "v"(voff), "s"(sbase), "s"(lds_base) : "memory");
}
__device__ __forceinline__ s16x4 trread(lds_cptr p) {
  typedef short v4i16_t __attribute__((ext_vector_type(4)));
  return __builtin_bit_cast(s16x4, __builtin_amdgcn_ds_read_tr16_b64_v4i16((LDS_AS v4i16_t*)p));
}
__device__ __forceinline__ bf16x8 cat8(s16x4 a, s16x4 b) {
  return (bf16x8){a[0], a[1], a[2], a[3], b[0], b[1], b[2], b[3]};
}
__device__ __forceinline__ bf16x8 ldsr128(lds_cptr p) { return *(const LDS_AS bf16x8*)p; }
#define MFMA16(a, b, c) __builtin_amdgcn_mfma_f32_16x16x32_bf16((a), (b), (c), 0, 0, 0)
#define MFMA32(a, b, c) __builtin_amdgcn_mfma_f32_32x32x16_bf16((a), (b), (c), 0, 0, 0)
#define SB0() __builtin_amdgcn_sched_barrier(0)
#define SGB(mask, n) __builtin_amdgcn_sched_group_barrier((mask), (n), 0)
#define WAITV(n) asm volatile("s_waitcnt vmcnt(" #n ")" ::: "memory")
#define BARRIER() do { asm volatile("" ::: "memory"); __builtin_amdgcn_s_waitcnt(0xC07F); __builtin_amdgcn_s_barrier(); asm volatile("" ::: "memory"); } while (0)
__device__ __forceinline__ float dot2(unsigned a, unsigned b, float c) {
  return __builtin_amdgcn_fdot2_f32_bf16(__builtin_bit_cast(bf16x2_t, a), __builtin_bit_cast(bf16x2_t, b), c, false);
}
constexpr unsigned ONES2 = 0x3F803F80u;
__device__ __forceinline__ float sel4(float a, float b, float c, float d, unsigned m0, unsigned m1, unsigned m2, unsigned m3) {
  return __uint_as_float((__float_as_uint(a) & m0) | (__float_as_uint(b) & m1) | (__float_as_uint(c) & m2) | (__float_as_uint(d) & m3));
}
__device__ __forceinline__ unsigned eqmask(int a, int b) { unsigned m = (a == b) ? ~0u : 0u; asm volatile("" : "+v"(m)); return m; }
#define STAMP(kid, i) do {} while (0)
#define TSTAMP(i) do {} while (0)
#define TFLUSH(kid, a, b) do {} while (0)

#define CODE_ADDR(dst, sym) do { unsigned lo_, hi_; \
    asm volatile("s_getpc_b64 s[98:99]\n\ts_add_u32 %0, s98, " sym "@rel32@lo+4\n\ts_addc_u32 %1, s99, " sym "@rel32@hi+12" \
                 : "=s"(lo_), "=s"(hi_) :: "s98", "s99", "scc"); \
    dst = (const char*)(((unsigned long long)hi_ << 32) | lo_); } while (0)

#define CODE_TOUCH(symA, symB, slot, ldsd) do { const char* ca_; const char* cb_; CODE_ADDR(ca_, symA); CODE_ADDR(cb_, symB); \
    const unsigned ns_ = (unsigned)(cb_ - ca_) >> 6; \
    for (unsigned c0_ = (unsigned)(slot) * 64u; c0_ < ns_; c0_ += 512u) { const unsigned ci_ = c0_ + lane; touch4(ca_, (ci_ < ns_ ? ci_ : 0u) * 64u, (ldsd)); } } while (0)
#define SYM_K1 "_Z6k1_qkvPKtS0_S0_Pc"
#define SYM_K2 "_Z7k2_attnPcPKtS1_"
#define SYM_K3 "_Z7k3_projPKtPc"
#define SYM_K4 "_Z8k4_gemm1PKtS0_S0_Pc"
#define SYM_K5 "_Z8k5_gemm2PKtS0_PcPt"
#define SYM_END "_Z5k_endv"

constexpr int Q_ST = 5, Q_SB = 30720;
__global__ __launch_bounds__(512, 1) void k1_qkv(const u16* __restrict__ x, const u16* __restrict__ wqkv, const u16* __restrict__ wo,
                                                  char* __restrict__ ws) {
  __shared__ __attribute__((aligned(16))) char lds[Q_ST * Q_SB + 512 + 1024];
  const int tid = threadIdx.x, lane = tid & 63;
  const int w8 = __builtin_amdgcn_readfirstlane(tid >> 6), w = w8 & 3;
  const int b = blockIdx.x, xg = b & 7, o = b >> 3;
  const int bm = (xg >> 1) * 4 + (o & 3), hg = (xg & 1) * 8 + (o >> 2);
  const unsigned lds0 = (unsigned)(uintptr_t)lds;
  float* csl = (float*)(lds + Q_ST * Q_SB);
  constexpr unsigned DUMMY = Q_ST * Q_SB + 512;
  constexpr int SP = 304;
  u16 wog[8];
  STAMP(0, 0);
  if (w8 >= 4) {
    const char* xb = (const char*)x + (size_t)bm * 128 * 1152;
    const char* wb = (const char*)wqkv + (216 * hg - 8 * (hg & 1));
    unsigned xoff[4], woff[4];
#pragma unroll
    for (int i = 0; i < 4; ++i) {
      const int row = (w + 4 * i) * 8 + (lane >> 3);
      xoff[i] = (unsigned)(row * 1152 + (((lane & 7) ^ (lane >> 3)) * 16));
      int pc = w + 4 * i; if (pc >= 14) pc -= 4;
      const unsigned c = (unsigned)(pc * 64 + lane), pos = c / 14u, ch = c - pos * 14u;
      const unsigned k = (pos & ~12u) | ((pos & 4u) << 1) | ((pos & 8u) >> 1);
      woff[i] = k * 3456u + ch * 16u;
    }
    int it_ = 0; unsigned isl = lds0;
#define Q_ISSUE() do { const char* xb_ = xb + (size_t)it_ * 128; const char* wb_ = wb + (size_t)it_ * (64 * 3456); \
    _Pragma("unroll") for (int i = 0; i < 3; ++i) glds16(wb_, woff[i], isl + 16384u + (unsigned)(w + 4 * i) * 1024u); \
    if (w < 2) glds16(wb_, woff[3], isl + 16384u + (unsigned)(w + 12) * 1024u); \
    _Pragma("unroll") for (int i = 0; i < 4; ++i) glds16(xb_, xoff[i], isl + (unsigned)(w + 4 * i) * 1024u); \
    ++it_; isl = (isl + Q_SB == lds0 + Q_ST * Q_SB) ? lds0 : isl + Q_SB; } while (0)
#define Q_WAIT(NA, NB) do { if (w < 2) asm volatile("s_waitcnt vmcnt(" #NA ")\n\ts_barrier" ::: "memory"); \
                            else       asm volatile("s_waitcnt vmcnt(" #NB ")\n\ts_barrier" ::: "memory"); } while (0)
    Q_ISSUE(); Q_ISSUE(); Q_ISSUE();
    Q_WAIT(16, 14); Q_ISSUE(); Q_ISSUE();
#pragma unroll 1
    for (int t = 1; t < 5; ++t) { Q_WAIT(24, 21); Q_ISSUE(); }
    {
      const int tk = b * 162 + ((tid - 256 < 162) ? tid - 256 : 0);
      const int l_ = tk & 63, ks = (tk >> 6) % 18, nt = tk / (64 * 18);
      const int g_ = l_ >> 4, n_ = nt * 16 + (l_ & 15);
      const int k0 = (ks < 16) ? 18 * (2 * ks + (g_ >> 1)) + 4 * (g_ & 1) : 18 * (16 * (ks - 16) + 4 * g_) + 16;
      const u16* wp_ = wo + k0 * 576 + n_;
#pragma unroll
      for (int j = 0; j < 8; ++j) {
        const int dk = (ks < 16) ? (j & 3) + 8 * (j >> 2) : 18 * (j >> 1) + (j & 1);
        wog[j] = wp_[dk * 576];
      }
    }
    Q_WAIT(32, 29);
    Q_WAIT(24, 22);
    Q_WAIT(16, 15);
    Q_WAIT(8, 8);
    if (tid - 256 < 32) *(u32x4*)(ws + WS_GRAN + (size_t)b * 512 + (tid - 256) * 16) = (u32x4){0, 0, 0, 0};
    BARRIER();
    BARRIER();
  } else {
    const unsigned vw = (unsigned)(o * 4 + w);
    CODE_TOUCH(SYM_K1, SYM_K3, vw & 7u, lds0 + DUMMY);
    const int g = lane >> 4, c16 = lane & 15, q4 = (lane & 15) >> 2, p4 = lane & 3;
#define Q_TILE(i) ((w == 3) ? ((i) == 0 ? 6 : (i) - 1) : ((i) < 2 ? 2 * w + (i) : ((i) < 2 + 2 * w ? (i) - 2 : (i))))
    unsigned wrd[7], so[7][2]; float qs[7][2];
#pragma unroll
    for (int i = 0; i < 7; ++i) {
      const int n = Q_TILE(i);
      int Q = 4 * n + p4; if (Q > 26) Q = 26;
      wrd[i] = (unsigned)(16384 + ((g >> 1) * 16 + (g & 1) * 4 + q4) * 224 + 8 * Q + 8 * (hg & 1));
#pragma unroll
      for (int e = 0; e < 2; ++e) {
        const unsigned col = (unsigned)(n * 16 + g * 4 + e * 2);
        const unsigned hl = col / 18u;
        so[i][e] = (unsigned)((w * 32 + c16) * SP) + col * 2u + hl * 12u;
        qs[i][e] = ((unsigned)hg * 6u + hl < 32u) ? QSCALE : 1.0f;
        asm volatile("" : "+v"(so[i][e]), "+v"(qs[i][e]));
      }
    }
    const unsigned xrd0 = (unsigned)((w * 32 + c16) * 128 + ((g ^ (c16 & 7)) * 16)), xrd1 = xrd0 ^ 64u;
    f32x4 acc[7][2], csa[2], sq[2], sx[2];
#pragma unroll
    for (int n = 0; n < 7; ++n) { acc[n][0] = (f32x4){0, 0, 0, 0}; acc[n][1] = (f32x4){0, 0, 0, 0}; }
#pragma unroll
    for (int i = 0; i < 2; ++i) { csa[i] = (f32x4){0, 0, 0, 0}; sq[i] = (f32x4){0, 0, 0, 0}; sx[i] = (f32x4){0, 0, 0, 0}; }
    const bf16x8 ones8 = __builtin_bit_cast(bf16x8, (u32x4){ONES2, ONES2, ONES2, ONES2});
    bf16x8 wX[7], xX[2], wY[7], xY[2];
    int slot = 0;
#define Q_READ(W_, X_, ks) do { const lds_cptr sp_ = (lds_cptr)lds + slot * Q_SB; \
    X_[0] = ldsr128(sp_ + ((ks) ? xrd1 : xrd0)); X_[1] = ldsr128(sp_ + ((ks) ? xrd1 : xrd0) + 2048); \
    _Pragma("unroll") for (int n = 0; n < 7; ++n) W_[n] = cat8(trread(sp_ + wrd[n] + (ks) * 7168), trread(sp_ + wrd[n] + (ks) * 7168 + 1792)); } while (0)
#define Q_MMA(W_, X_) do { \
    sx[0] = MFMA16(ones8, X_[0], sx[0]); sx[1] = MFMA16(ones8, X_[1], sx[1]); \
    sq[0] = MFMA16(X_[0], X_[0], sq[0]); sq[1] = MFMA16(X_[1], X_[1], sq[1]); \
    _Pragma("unroll") for (int n = 0; n < 7; ++n) { acc[n][0] = MFMA16(W_[n], X_[0], acc[n][0]); acc[n][1] = MFMA16(W_[n], X_[1], acc[n][1]); \
      if (n < 2) csa[n] = MFMA16(W_[n], ones8, csa[n]); } } while (0)
#define Q_ILV() do { _Pragma("unroll") for (int i_ = 0; i_ < 16; ++i_) { SGB(0x8, 1); SGB(0x100, 1); } SGB(0x8, 4); } while (0)
#define TDELAY 16
    {
      __builtin_amdgcn_s_sleep(TDELAY);
      unsigned i = vw * 64u + lane, rw = i / 27u;
      const char* wq = (const char*)wqkv + 192 * 3456 + (xg & 1) * 1728;
      touch4(wq, rw * 3456u + (i - rw * 27u) * 64u, lds0 + DUMMY);
      if (vw < 96u) { rw = i / 12u; touch4((const char*)x + (size_t)(xg >> 1) * 589824 + 384, rw * 1152u + (i - rw * 12u) * 64u, lds0 + DUMMY); }
      else { i += 2048u; rw = i / 27u; touch4(wq, rw * 3456u + (i - rw * 27u) * 64u, lds0 + DUMMY); }
      if (vw < 2u) { i = 10240u + vw * 64u + lane; rw = i / 27u; touch4(wq, rw * 3456u + (i - rw * 27u) * 64u, lds0 + DUMMY); }
    }
    asm volatile("" :: "s"(ws));
    BARRIER();
    STAMP(0, 1);
    __builtin_amdgcn_s_waitcnt(0xC07F);
    Q_READ(wX, xX, 0);
    SB0();
#pragma unroll
    for (int t = 0; t < 9; ++t) {
      Q_READ(wY, xY, 1); Q_MMA(wX, xX); Q_ILV(); SB0();
      if (t < 8) {
        BARRIER(); slot = (slot + 1 == Q_ST) ? 0 : slot + 1;
        SB0();
        Q_READ(wX, xX, 0); Q_MMA(wY, xY); Q_ILV(); SB0();
      } else { Q_MMA(wY, xY); }
    }
    STAMP(0, 2);
    if (c16 == 0) { *(f32x4*)(csl + (2 * w) * 16 + g * 4) = csa[0]; if (w < 3) *(f32x4*)(csl + (2 * w + 1) * 16 + g * 4) = csa[1]; }
    float rstd[2], mr[2];
    const unsigned jm0 = eqmask(c16 & 3, 0), jm1 = eqmask(c16 & 3, 1), jm2 = eqmask(c16 & 3, 2), jm3 = eqmask(c16 & 3, 3);
#pragma unroll
    for (int tt = 0; tt < 2; ++tt) {
      const float dg = sel4(sq[tt][0], sq[tt][1], sq[tt][2], sq[tt][3], jm0, jm1, jm2, jm3);
      const float ss = __shfl(dg, (c16 >> 2) * 16 + c16);
      const float mean = sx[tt][0] * (1.f / 576.f);
      rstd[tt] = rsqrtf(fmaxf(ss * (1.f / 576.f) - mean * mean, 0.f) + LN_EPS);
      mr[tt] = -mean;
    }
    BARRIER();
#pragma unroll
    for (int n = 0; n < 7; ++n) {
      const int nt = Q_TILE(n);
      const f32x4 cs4 = *(const f32x4*)(csl + nt * 16 + g * 4);
#pragma unroll
      for (int e = 0; e < 2; ++e)
#pragma unroll
        for (int tt = 0; tt < 2; ++tt) {
          const float rs = rstd[tt] * qs[n][e];
          const float v0 = __builtin_fmaf(mr[tt], cs4[2 * e], acc[n][tt][2 * e]) * rs;
          const float v1 = __builtin_fmaf(mr[tt], cs4[2 * e + 1], acc[n][tt][2 * e + 1]) * rs;
          *(unsigned*)(lds + so[n][e] + tt * 16 * SP) = cvtpk(v0, v1);
        }
    }
    BARRIER();
  }
  STAMP(0, 3);
  {
    const unsigned t8 = (unsigned)tid & 255u;
    const unsigned Tl = t8 >> 1, c = t8 & 1u, T = (unsigned)bm * 128u + Tl;
    const unsigned oQK = (T >> 5) * 1024u + c * 512u + (T & 31u) * 16u;
    const unsigned T2 = (unsigned)bm * 128u + (t8 & 127u);
    const unsigned oKX = KXOFF + (T2 >> 7) * 512u + (T2 & 31u) * 16u + ((T2 >> 5) & 3u) * 4u;
    const unsigned vrec = t8 >> 5, vhh = (t8 >> 4) & 1u, vi = t8 & 15u;
    const lds_cptr vsrc = (lds_cptr)lds + ((vrec * 16u + vhh * 4u + (vi >> 2)) * SP + (vi & 3u) * 8u);
    const lds_cptr vsrx = (lds_cptr)lds + ((vrec * 16u + vhh * 4u + (vi >> 2)) * SP + 32u);
    const unsigned oV = 65536u + ((unsigned)bm * 8u + vrec) * VREC + vhh * 320u + vi * 16u;
#pragma unroll
    for (int i = 0; i < 3; ++i) {
      const int hl = (w8 >> 2) * 3 + i;
      const int hs = hg * 6 + hl, which = hs >> 5, h = hs & 31;
      char* base = ws + (which == 0 ? WS_QM + (size_t)h * 65536 : WS_KV + (size_t)h * KVHEAD);
      if (which == 2) {
        const bf16x8 vm = cat8(trread(vsrc + hl * 48), trread(vsrc + hl * 48 + 8 * SP));
        bf16x8 vx = cat8(trread(vsrx + hl * 48), trread(vsrx + hl * 48 + 8 * SP));
        *(bf16x8*)(base + oV) = vm;
        if (vi == 2u) vx = __builtin_bit_cast(bf16x8, (u32x4){ONES2, ONES2, ONES2, ONES2});
        if (vi == 3u) vx = (bf16x8){0, 0, 0, 0, 0, 0, 0, 0};
        if (vi < 4u) *(bf16x8*)(base + oV + 256u) = vx;
      } else {
        *(u32x4*)(base + oQK) = *(const u32x4*)(lds + Tl * SP + hl * 48 + c * 16);
        if (t8 < 128u) {
          const unsigned xv = *(const unsigned*)(lds + t8 * SP + hl * 48 + 32);
          if (which == 0) *(unsigned*)(ws + WS_QX + (size_t)h * 8192 + T2 * 4u) = xv;
          else *(unsigned*)(base + oKX) = xv;
        }
      }
    }
  }
  if (tid >= 256 && tid - 256 < 162) {
    u32x4 v;
#pragma unroll
    for (int j = 0; j < 4; ++j) v[j] = (unsigned)wog[2 * j] | ((unsigned)wog[2 * j + 1] << 16);
    *(u32x4*)(ws + WS_WOF + (size_t)(b * 162 + tid - 256) * 16) = v;
  }
  STAMP(0, 4);
  asm volatile("s_waitcnt vmcnt(0)" ::: "memory");
#undef Q_ISSUE
#undef Q_TILE
#undef Q_WAIT
#undef Q_READ
#undef Q_MMA
#undef Q_ILV
}

#define NW2 7
#define HTHR 6
#define STR2_(x) #x
#define STR_(x) STR2_(x)
#define EX2(v) __builtin_amdgcn_exp2f(v)
__global__ __launch_bounds__(512, 1) void k2_attn(char* __restrict__ ws, const u16* __restrict__ w1, const u16* __restrict__ w2) {
  __shared__ __attribute__((aligned(16))) char lds[KVHEAD + 1024];
  const int tid = threadIdx.x, lane = tid & 63;
  const int w = __builtin_amdgcn_readfirstlane(tid >> 6);
  const int b = blockIdx.x, xg = b & 7;
  const int h = xg * 4 + ((b >> 3) & 3), qt = b >> 5;
  const int qp = w & 3, half = w >> 2;
  const unsigned lds0 = (unsigned)(uintptr_t)lds;
  const int r = lane & 31, hh = lane >> 5;
  const char* kvg = ws + WS_KV + (size_t)h * KVHEAD;
  const unsigned l16 = (unsigned)lane * 16u;
  STAMP(1, 0);
  u32x4 qmA, qmB; unsigned qxA, qxB;
  {
    const char* qmp = ws + WS_QM + (size_t)h * 65536 + (size_t)(qt * 8 + qp * 2) * 1024 + lane * 16;
    const char* qxp = ws + WS_QX + (size_t)h * 8192 + (size_t)(qt * 256 + qp * 64 + r) * 4;
    asm volatile("global_load_dwordx4 %0, %1, off" : "=v"(qmA) : "v"(qmp) : "memory");
    asm volatile("global_load_dwordx4 %0, %1, off offset:1024" : "=v"(qmB) : "v"(qmp) : "memory");
    asm volatile("global_load_dword %0, %1, off" : "=v"(qxA) : "v"(qxp) : "memory");
    asm volatile("global_load_dword %0, %1, off offset:128" : "=v"(qxB) : "v"(qxp) : "memory");
  }
#pragma unroll
  for (int i = 0; i < 6; ++i) {
    const int s = i * 8 + w;
    const int p = (s < 10) ? s : (s < 20) ? 32 + s - 10 : (s < 30) ? 64 + s - 20 : (s < 40) ? 104 + s - 30 : 144 + s - 40;
    glds16(kvg + (size_t)p * 1024, l16, lds0 + p * 1024);
  }
#define STAGEB(i) do { const int s_ = (i) * 8 + w; \
    const int p_ = (s_ < 22) ? 10 + s_ : (s_ < 44) ? 42 + s_ - 22 : (s_ < 74) ? 74 + s_ - 44 : 114 + s_ - 74; \
    glds16(kvg + (size_t)p_ * 1024, l16, lds0 + p_ * 1024); } while (0)
  CODE_TOUCH(SYM_K2, SYM_K3, w, lds0 + KVHEAD);
  const lds_cptr vp = (lds_cptr)lds + (65536u + (unsigned)half * 64u * VREC + hh * 320 + (r < 19 ? r : 19) * 16);
  const lds_cptr kp = (lds_cptr)lds + (half * 32768 + hh * 512 + r * 16);
  const lds_cptr xp = (lds_cptr)lds + (KXOFF + half * 4096 + r * 16);

  const char* pfb = (const char*)w1 + (size_t)(b * 8 + w) * 1024;
  asm volatile("s_waitcnt vmcnt(0)\n\ts_barrier" : "+v"(qmA), "+v"(qmB), "+v"(qxA), "+v"(qxB) :: "memory");
  STAMP(1, 1);
  const bf16x8 qA = __builtin_bit_cast(bf16x8, qmA), qB = __builtin_bit_cast(bf16x8, qmB);
  bf16x8 eA[4], eB[4];
  {
    const unsigned a = hh == 0 ? qxA : 0u, bq = hh == 0 ? qxB : 0u;
    eA[0] = __builtin_bit_cast(bf16x8, (u32x4){a, 0u, 0u, 0u}); eB[0] = __builtin_bit_cast(bf16x8, (u32x4){bq, 0u, 0u, 0u});
    eA[1] = __builtin_bit_cast(bf16x8, (u32x4){0u, a, 0u, 0u}); eB[1] = __builtin_bit_cast(bf16x8, (u32x4){0u, bq, 0u, 0u});
    eA[2] = __builtin_bit_cast(bf16x8, (u32x4){0u, 0u, a, 0u}); eB[2] = __builtin_bit_cast(bf16x8, (u32x4){0u, 0u, bq, 0u});
    eA[3] = __builtin_bit_cast(bf16x8, (u32x4){0u, 0u, 0u, a}); eB[3] = __builtin_bit_cast(bf16x8, (u32x4){0u, 0u, 0u, bq});
  }
  const f32x16 zero16 = {};
  f32x16 oA = {}, oB = {}, sA, sB;
  u32x4 pAl = {0u, 0u, 0u, 0u}, pAh = {0u, 0u, 0u, 0u}, pBl = {0u, 0u, 0u, 0u}, pBh = {0u, 0u, 0u, 0u};
  bf16x8 kf, kx, kxn, v0, v1 = {};
  kf = ldsr128(kp); kx = ldsr128(xp);
  sA = MFMA32(kf, qA, zero16); sA = MFMA32(kx, eA[0], sA);
  float e0 = 0.f, e1 = 0.f, e2 = 0.f, e3 = 0.f, f0, f1, f2, f3;
  SB0();
#define EX4(a, b_, c_, d_, S, i0) do { a = EX2(S[i0]); b_ = EX2(S[i0 + 1]); c_ = EX2(S[i0 + 2]); d_ = EX2(S[i0 + 3]); } while (0)
#define CV2(P, j0, a, b_, c_, d_) do { P[j0] = cvtpk(a, b_); P[j0 + 1] = cvtpk(c_, d_); } while (0)
#define PF8(x) __builtin_bit_cast(bf16x8, x)
#define PIN(x) asm volatile("" : "+v"(x))
#define PINE() asm volatile("" : "+v"(e0), "+v"(e1), "+v"(e2), "+v"(e3))
#define PINF() asm volatile("" : "+v"(f0), "+v"(f1), "+v"(f2), "+v"(f3))
#define EXA(a, b_, S, i0) do { a = EX2(S[i0]); b_ = EX2(S[i0 + 1]); asm volatile("" : "+v"(a), "+v"(b_)); SB0(); } while (0)
#define EXB(c_, d_, S, i0) do { c_ = EX2(S[i0 + 2]); d_ = EX2(S[i0 + 3]); } while (0)
#define TILE(t) do { \
    constexpr int ro_ = (t) * 2 * (int)VREC; \
      sB = MFMA32(kf, qB, zero16); PIN(sB); SB0(); v0 = ldsr128(vq + ro_); kf = ldsr128(kq + ((t) + 1) * 1024); \
           if ((t) == 3) { kxn = ldsr128(xq + 512); } \
           EX4(f0, f1, f2, f3, sA, 0); CV2(pBh, 2, e0, e1, e2, e3); PINF(); PIN(pBh); SB0(); \
      EXA(e0, e1, sA, 4); oB = MFMA32(v1, PF8(pBh), oB); PIN(oB); SB0(); v1 = ldsr128(vq + ro_ + VREC); \
           EXB(e2, e3, sA, 4); CV2(pAl, 0, f0, f1, f2, f3); PINE(); PIN(pAl); SB0(); \
      EXA(f0, f1, sA, 8); sB = MFMA32(kx, eB[t], sB); PIN(sB); SB0(); \
           EXB(f2, f3, sA, 8); CV2(pAl, 2, e0, e1, e2, e3); PINF(); PIN(pAl); SB0(); \
      EXA(e0, e1, sA, 12); __builtin_amdgcn_s_waitcnt(0xC07F); SB0(); oA = MFMA32(v0, PF8(pAl), oA); PIN(oA); SB0(); \
           EXB(e2, e3, sA, 12); CV2(pAh, 0, f0, f1, f2, f3); PINE(); PIN(pAh); SB0(); \
      EXA(f0, f1, sB, 0); sA = MFMA32(kf, qA, zero16); PIN(sA); SB0(); EXB(f2, f3, sB, 0); CV2(pAh, 2, e0, e1, e2, e3); PINF(); PIN(pAh); SB0(); \
      EXA(e0, e1, sB, 4); oA = MFMA32(v1, PF8(pAh), oA); PIN(oA); SB0(); EXB(e2, e3, sB, 4); CV2(pBl, 0, f0, f1, f2, f3); PINE(); PIN(pBl); SB0(); \
      EXA(f0, f1, sB, 8); sA = MFMA32((t) == 3 ? kxn : kx, eA[((t) + 1) & 3], sA); PIN(sA); SB0(); EXB(f2, f3, sB, 8); CV2(pBl, 2, e0, e1, e2, e3); PINF(); PIN(pBl); SB0(); \
      EXA(e0, e1, sB, 12); oB = MFMA32(v0, PF8(pBl), oB); PIN(oB); SB0(); EXB(e2, e3, sB, 12); CV2(pBh, 0, f0, f1, f2, f3); PINE(); PIN(pBh); SB0(); \
  } while (0)
#define SIDE(a) do { STAGEB(it * 8 + (a)); if (it * 8 + (a) + 1 < 13) STAGEB(it * 8 + (a) + 1); SB0(); } while (0)
#define PFETCH() do { glds16(pfb, l16, lds0 + KVHEAD); pfb += 2048 * 1024; if (++pfn == 9) pfb = (const char*)w2 + (size_t)(b * 8 + w) * 1024; SB0(); } while (0)
  int pfn = 0;
  lds_cptr vq = vp, kq = kp, xq = xp;
  const auto slabr = __builtin_amdgcn_make_buffer_rsrc(ws + WS_SLAB + (size_t)b * 40960, 0, 40960, 0x00020000);
  __builtin_amdgcn_s_setprio(3);
#pragma unroll 1
  for (int it = 0; it < 2; ++it) {
    SIDE(0); TILE(0);
    SIDE(2); TILE(1);
    SIDE(4); TILE(2);
    if (it == 0) SIDE(6);
    TILE(3);
    kx = kxn;
    vq += 8 * VREC; kq += 4096; xq += 512;
  }
  asm volatile("s_waitcnt vmcnt(0)\n\ts_barrier" ::: "memory");
  STAMP(1, 2);
  if (b < 8) { CODE_TOUCH(SYM_K3, SYM_END, w, lds0 + KVHEAD); }
#pragma unroll 1
  for (int it = 2; it < 8; ++it) {
    if (it == 4) __builtin_amdgcn_s_setprio(2);
    if (it == 6) __builtin_amdgcn_s_setprio(1);
    if (it == 7) __builtin_amdgcn_s_setprio(0);
    SB0();
    PFETCH(); TILE(0);
    if (it >= 11 - NW2) { PFETCH(); }
    TILE(1);
    PFETCH();
    TILE(2);
    if (it < 7) {
      __builtin_amdgcn_raw_buffer_store_b128((u32x4){~0u, ~0u, ~0u, ~0u}, slabr, (w * 5 + it - 2) * 1024 + lane * 16, 0, 16);
      SB0();
    }
    TILE(3);
    kx = kxn;
    vq += 8 * VREC; kq += 4096; xq += 512;
  }
  CV2(pBh, 2, e0, e1, e2, e3);
  oB = MFMA32(v1, PF8(pBh), oB);
  STAMP(1, 3);

  float mA = 0.f, mB = 0.f;
  {
    bool bad = false;
    if (hh == 0) {
      bad = !(oA[10] >= 8.271806125530277e-25f && oA[10] <= 1.2676506002282294e30f) ||
            !(oB[10] >= 8.271806125530277e-25f && oB[10] <= 1.2676506002282294e30f);
    }
#pragma unroll
    for (int i = 0; i < 10; ++i) bad |= !(__builtin_fabsf(oA[i]) <= 3.0e38f) || !(__builtin_fabsf(oB[i]) <= 3.0e38f);
    if (__any(bad)) {
#pragma unroll 1
      for (int qq = 0; qq < 2; ++qq) {
        const bf16x8 qf = qq ? qB : qA;
        f32x16 oacc = zero16;
        float m = -INFINITY;
        for (int it = 0; it < 8; ++it) {
          const bf16x8 kx_ = ldsr128(xp + it * 512);
#pragma unroll
          for (int t = 0; t < 4; ++t) {
            const bf16x8 kf_ = ldsr128(kp + (it * 4 + t) * 1024);
            f32x16 s_ = MFMA32(kf_, qf, zero16);
            s_ = MFMA32(kx_, qq ? eB[t] : eA[t], s_);
            float tm = s_[0];
#pragma unroll
            for (int i = 1; i < 16; ++i) tm = fmaxf(tm, s_[i]);
            tm = fmaxf(tm, __shfl_xor(tm, 32));
            const float mn = fmaxf(m, tm);
            const float sc = EX2(m - mn);
            m = mn;
#pragma unroll
            for (int i = 0; i < 16; ++i) oacc[i] *= sc;
            const int ro_ = (it * 8 + t * 2) * (int)VREC;
            const bf16x8 v0_ = ldsr128(vp + ro_), v1_ = ldsr128(vp + ro_ + VREC);
            u32x4 pa_, pb_;
#pragma unroll
            for (int i = 0; i < 4; ++i) {
              pa_[i] = cvtpk(EX2(s_[2 * i] - m), EX2(s_[2 * i + 1] - m));
              pb_[i] = cvtpk(EX2(s_[8 + 2 * i] - m), EX2(s_[8 + 2 * i + 1] - m));
            }
            oacc = MFMA32(v0_, PF8(pa_), oacc);
            oacc = MFMA32(v1_, PF8(pb_), oacc);
          }
        }
        if (qq) { oB = oacc; mB = m; } else { oA = oacc; mA = m; }
      }
    }
  }
  const unsigned hm0 = eqmask(half, 0), hm1 = eqmask(half, 1);
  BARRIER();
  {
    f32x4* mb = (f32x4*)(lds + w * 4096);
    float os[11];
#pragma unroll
    for (int i = 0; i < 11; ++i) os[i] = sel4(oA[i], oB[i], 0.f, 0.f, hm1, hm0, 0u, 0u);
    const float ms = sel4(mA, mB, 0.f, 0.f, hm1, hm0, 0u, 0u);
    mb[lane] = (f32x4){os[0], os[1], os[2], os[3]};
    mb[64 + lane] = (f32x4){os[4], os[5], os[6], os[7]};
    mb[128 + lane] = (f32x4){os[8], os[9], os[10], ms};
  }
  BARRIER();
  {
    const f32x4* mb = (const f32x4*)(lds + (w ^ 4) * 4096);
    float ok[11];
#pragma unroll
    for (int i = 0; i < 11; ++i) ok[i] = sel4(oA[i], oB[i], 0.f, 0.f, hm0, hm1, 0u, 0u);
    const float mk = sel4(mA, mB, 0.f, 0.f, hm0, hm1, 0u, 0u);
    const f32x4 a0 = mb[lane], a1 = mb[64 + lane], a2 = mb[128 + lane];
    const float M = fmaxf(mk, a2[3]);
    const float s0 = EX2(mk - M), s1 = EX2(a2[3] - M);
    const float lsum = __shfl(ok[10] * s0 + a2[2] * s1, r);
    const float inv = 1.0f / lsum;
    const float c0 = s0 * inv, c1 = s1 * inv;
    u32x4 ov;
    ov[0] = cvtpk(ok[0] * c0 + a0[0] * c1, ok[1] * c0 + a0[1] * c1);
    ov[1] = cvtpk(ok[2] * c0 + a0[2] * c1, ok[3] * c0 + a0[3] * c1);
    ov[2] = cvtpk(ok[4] * c0 + a1[0] * c1, ok[5] * c0 + a1[1] * c1);
    ov[3] = cvtpk(ok[6] * c0 + a1[2] * c1, ok[7] * c0 + a1[3] * c1);
    const int T = qt * 256 + qp * 64 + half * 32 + r;
    char* ofb = ws + WS_OF + (size_t)(T >> 4) * 18432;
    *(u32x4*)(ofb + (h >> 1) * 1024 + (((h & 1) * 2 + hh) * 16 + (T & 15)) * 16) = ov;
    if (hh == 0)
      *(unsigned*)(ofb + (16 + (h >> 4)) * 1024 + (((h & 15) >> 2) * 16 + (T & 15)) * 16 + (h & 3) * 4) =
          cvtpk(ok[8] * c0 + a2[0] * c1, ok[9] * c0 + a2[1] * c1);
  }
  STAMP(1, 4);
  asm volatile("s_waitcnt vmcnt(0)" ::: "memory");
#undef TILE
#undef SIDE
#undef PFETCH
#undef STAGEB
#undef EX4
#undef CV2
}

__global__ __launch_bounds__(576, 1) void k3_proj(const u16* __restrict__ x, char* __restrict__ ws) {
  __shared__ __attribute__((aligned(16))) char lds[36864 + 9 * 256 + 256];
  const int tid = threadIdx.x, lane = tid & 63;
  const int w = __builtin_amdgcn_readfirstlane(tid >> 6);
  const int b = blockIdx.x, xg = b & 7, o = b >> 3;
  const int tt32 = xg * 8 + (o >> 2), ct = o & 3;
  const int g = lane >> 4, c16 = lane & 15;
  const int nt = ct * 9 + w;
  const unsigned lds0 = (unsigned)(uintptr_t)lds;
  STAMP(2, 0);
#pragma unroll
  for (int i = 0; i < 4; ++i)
    glds16(ws + WS_OF + (size_t)(tt32 * 2) * 18432 + (size_t)(i * 9 + w) * 1024, (unsigned)lane * 16u, lds0 + (unsigned)(i * 9 + w) * 1024u);
  const char* wfp = ws + WS_WOF + (size_t)nt * 18432 + lane * 16;
  bf16x8 bfr[18];
#pragma unroll
  for (int ks = 0; ks < 6; ++ks) bfr[ks] = *(const bf16x8*)(wfp + ks * 1024);
  asm volatile("s_waitcnt vmcnt(6)\n\ts_barrier" ::: "memory");
#pragma unroll
  for (int ks = 6; ks < 18; ++ks) bfr[ks] = *(const bf16x8*)(wfp + ks * 1024);
  u16 xr[2][4];
#pragma unroll
  for (int t = 0; t < 2; ++t)
#pragma unroll
    for (int j = 0; j < 4; ++j) xr[t][j] = x[(size_t)(tt32 * 32 + t * 16 + g * 4 + j) * 576 + nt * 16 + c16];
  SB0();
  f32x4 acc[2], acd[2];
  acc[0] = acc[1] = acd[0] = acd[1] = (f32x4){0, 0, 0, 0};
#pragma unroll
  for (int ks = 0; ks < 18; ks += 2) {
    acc[0] = MFMA16(ldsr128((lds_cptr)lds + ks * 1024 + lane * 16), bfr[ks], acc[0]);
    acc[1] = MFMA16(ldsr128((lds_cptr)lds + 18432 + ks * 1024 + lane * 16), bfr[ks], acc[1]);
    acd[0] = MFMA16(ldsr128((lds_cptr)lds + (ks + 1) * 1024 + lane * 16), bfr[ks + 1], acd[0]);
    acd[1] = MFMA16(ldsr128((lds_cptr)lds + 18432 + (ks + 1) * 1024 + lane * 16), bfr[ks + 1], acd[1]);
  }
  float* hp = (float*)(ws + WS_H);
  float* pst = (float*)(lds + 36864);
  float* fin = (float*)(lds + 36864 + 9 * 256);
  float sm[8], m2[8];
#pragma unroll
  for (int i = 0; i < 8; ++i) {
    const float hv = acc[i >> 2][i & 3] + acd[i >> 2][i & 3] + bf2f(xr[i >> 2][i & 3]);
    acc[i >> 2][i & 3] = hv; sm[i] = hv;
  }
#define DPPADD(v, c) v += __builtin_bit_cast(float, __builtin_amdgcn_update_dpp(0, __builtin_bit_cast(int, v), c, 0xf, 0xf, false))
#define ROW16(a) do { _Pragma("unroll") for (int i = 0; i < 8; ++i) DPPADD(a[i], 0x128); SB0(); _Pragma("unroll") for (int i = 0; i < 8; ++i) DPPADD(a[i], 0x124); SB0(); \
    _Pragma("unroll") for (int i = 0; i < 8; ++i) DPPADD(a[i], 0x122); SB0(); _Pragma("unroll") for (int i = 0; i < 8; ++i) DPPADD(a[i], 0x121); SB0(); } while (0)
  SB0();
  ROW16(sm);
#pragma unroll
  for (int i = 0; i < 8; ++i) { const float d = acc[i >> 2][i & 3] - sm[i] * (1.f / 16.f); m2[i] = d * d; }
  SB0();
  ROW16(m2);
  if (c16 == 0) {
#pragma unroll
    for (int t = 0; t < 2; ++t) {
      *(f32x4*)(pst + (w * 32 + t * 16 + g * 4) * 2) = (f32x4){sm[4 * t], m2[4 * t], sm[4 * t + 1], m2[4 * t + 1]};
      *(f32x4*)(pst + (w * 32 + t * 16 + g * 4) * 2 + 4) = (f32x4){sm[4 * t + 2], m2[4 * t + 2], sm[4 * t + 3], m2[4 * t + 3]};
    }
  }
#undef ROW16
#undef DPPADD
  STAMP(2, 1);
  BARRIER();
#define HSTORE() do { float* tb_ = (float*)(lds + w * 2048); \
    _Pragma("unroll") for (int i = 0; i < 8; ++i) tb_[(i >> 2) * 256 + ((c16 >> 2) * 16 + g * 4 + (i & 3)) * 4 + (c16 & 3)] = acc[i >> 2][i & 3]; \
    _Pragma("unroll") for (int t = 0; t < 2; ++t) \
      *(f32x4*)(hp + (size_t)((tt32 * 2 + t) * 36 + nt) * 256 + lane * 4) = *(const f32x4*)(tb_ + t * 256 + lane * 4); } while (0)
  if (w != 0) HSTORE();
  if (w == 0) {
    const int tok = lane & 31, st = lane >> 5;
    float sw[9], S = 0.f, M2 = 0.f;
#pragma unroll
    for (int i = 0; i < 9; ++i) { sw[i] = pst[(i * 32 + tok) * 2]; M2 += pst[(i * 32 + tok) * 2 + 1]; S += sw[i]; }
    const float mub = S * (1.f / 144.f);
#pragma unroll
    for (int i = 0; i < 9; ++i) { const float d = sw[i] * (1.f / 16.f) - mub; M2 += 16.f * d * d; }
    gu64* gr = (gu64*)(ws + WS_GRAN + (size_t)tt32 * 2048);
    __hip_atomic_store(gr + ct * 64 + lane, (1ull << 32) | (unsigned long long)__float_as_uint(st ? M2 : S), RLX_AGENT);
    float pv[4];
    for (unsigned spins = 0;; ++spins) {
      bool ok = true;
#pragma unroll
      for (int i = 0; i < 4; ++i) {
        const unsigned long long v = __hip_atomic_load(gr + i * 64 + lane, RLX_AGENT);
        pv[i] = __uint_as_float((unsigned)v);
        ok &= (v >> 32) == 1ull;
      }
      if (__all(ok) || spins > (1u << 22)) break;
      __builtin_amdgcn_s_sleep(1);
    }
    const float tot = (pv[0] + pv[1]) + (pv[2] + pv[3]);
    const float mu = tot * (1.f / 576.f);
    float extra = 0.f;
#pragma unroll
    for (int i = 0; i < 4; ++i) { const float d = pv[i] * (1.f / 144.f) - mu; extra += 144.f * d * d; }
    const float other = __shfl_xor(tot, 32);
    const float var = (other + extra) * (1.f / 576.f);
    const float r1 = rsqrtf(var + LN_EPS);
    const float r2 = rsqrtf(var * r1 * r1 + LN_EPS);
    if (st == 0) { fin[tok * 2] = mu; fin[tok * 2 + 1] = r1 * r2; }
  }
  BARRIER();
  STAMP(2, 2);
  char* hnp = ws + WS_HN;
#pragma unroll
  for (int t = 0; t < 2; ++t) {
    const f32x4 f0 = *(const f32x4*)(fin + (t * 16 + g * 4) * 2), f1 = *(const f32x4*)(fin + (t * 16 + g * 4) * 2 + 4);
    u32x2 v;
    v[0] = cvtpk((acc[t][0] - f0[0]) * f0[1], (acc[t][1] - f0[2]) * f0[3]);
    v[1] = cvtpk((acc[t][2] - f1[0]) * f1[1], (acc[t][3] - f1[2]) * f1[3]);
    *(u32x2*)(hnp + ((size_t)(nt * 64 + tt32) * 64 + (2 * t + (g >> 1)) * 16 + c16) * 16 + (g & 1) * 8) = v;
  }
  if (w == 0) HSTORE();
  STAMP(2, 3);
#undef HSTORE
}

__device__ __forceinline__ float gelu_tanh(float v) {
  const float z = v * (0.7978845608028654f + 0.035677408136300125f * v * v);
  const float e = __builtin_amdgcn_exp2f(z * -2.8853900817779268f);
  return v * __builtin_amdgcn_rcpf(1.0f + e);
}
constexpr int G1_SB = 28672;
template <int MT>
__device__ __forceinline__ void g1_body(char* lds, const u16* __restrict__ w1, const u16* __restrict__ b1, char* __restrict__ ws,
                                        int lane, int w8, int dt0, int pn) {
  const int w = w8 & 3;
  const unsigned lds0 = (unsigned)(uintptr_t)lds;
  const int wr = w >> 1, wc = w & 1;
  const int g = lane >> 4, c16 = lane & 15;
  f32x4 acc[4][MT];
  if (w8 >= 4) {
    const char* hn = ws + WS_HN + (size_t)dt0 * 65536;
    unsigned aoff[MT];
#pragma unroll
    for (int i = 0; i < MT; ++i) { const int a = w + 4 * i; aoff[i] = (unsigned)((a >> 1) * 65536 + (a & 1) * 1024 + lane * 16); }
    const char* wb = (const char*)w1 + (size_t)((pn >> 2) * 8 + (pn & 3)) * 128;
    unsigned boff[4];
#pragma unroll
    for (int i = 0; i < 4; ++i) {
      const int row = (4 * w + i) * 4 + (lane >> 4), cp = lane & 15;
      const int f = (row & 3) | (((row >> 3) & 1) << 2);
      const int ch = (((cp >> 1) ^ f) << 1) | (cp & 1);
      boff[i] = (unsigned)(row * 9216 + (ch & 7) * 16 + (ch >> 3) * 512);
    }
#define G1_ISSUE(t) do { const unsigned sl_ = lds0 + (unsigned)((t) & 3) * G1_SB; \
    const char* ab_ = hn + (size_t)(t) * 2048; const char* bb_ = wb + (size_t)(t) * (64 * 9216); \
    _Pragma("unroll") for (int i = 0; i < 4; ++i) glds16(bb_, boff[i], sl_ + 12288u + (unsigned)(4 * w + i) * 1024u); \
    _Pragma("unroll") for (int i = 0; i < MT; ++i) glds16(ab_, aoff[i], sl_ + (unsigned)(w + 4 * i) * 1024u); } while (0)
#define G1_WAIT(n) asm volatile("s_waitcnt vmcnt(%0)\n\ts_barrier" :: "i"(n) : "memory")
    G1_ISSUE(0); G1_ISSUE(1); G1_ISSUE(2);
#pragma unroll 1
    for (int t = 0; t < 29; ++t) { G1_WAIT(2 * (4 + MT)); G1_ISSUE(t + 3); }
    G1_WAIT(2 * (4 + MT)); G1_WAIT(4 + MT); G1_WAIT(0);
    BARRIER();
    BARRIER();
#pragma unroll
    for (int n = 0; n < 2; ++n)
#pragma unroll
      for (int m = 0; m < MT; ++m) acc[n][m] = *(const f32x4*)(lds + ((w * 2 + n) * MT + m) * 1024 + lane * 16);
  } else {
    CODE_TOUCH(SYM_K4, SYM_K5, w + 4 * (dt0 & 1), lds0 + 4 * G1_SB);
    const int q4 = (lane & 15) >> 2, p4 = lane & 3;
    const int fl = q4 | ((g & 1) << 2);
    lds_cptr bp0[4], bp1[4];
#pragma unroll
    for (int n = 0; n < 4; ++n) {
      bp0[n] = (lds_cptr)lds + (12288 + (8 * g + q4) * 256 + (((wc * 4 + n) ^ fl) * 32) + 8 * p4);
      bp1[n] = bp0[n] + 2 * G1_SB;
      asm volatile("" : "+v"(bp1[n]));
    }
    const lds_cptr ap0 = (lds_cptr)lds + (wr * MT * 2048 + lane * 16);
    lds_cptr ap1 = ap0 + 2 * G1_SB;
    asm volatile("" : "+v"(ap1));
#pragma unroll
    for (int n = 0; n < 4; ++n)
#pragma unroll
      for (int m = 0; m < MT; ++m) acc[n][m] = (f32x4){0, 0, 0, 0};
    bf16x8 aX[MT], bX[4], aY[MT], bY[4];
#define G1_READ(A_, B_, sl, ks) do { const lds_cptr a_ = ((sl) < 2 ? ap0 : ap1) + ((sl) & 1) * G1_SB + (ks) * 1024; \
    _Pragma("unroll") for (int m = 0; m < MT; ++m) A_[m] = ldsr128(a_ + m * 2048); \
    _Pragma("unroll") for (int n = 0; n < 4; ++n) { const lds_cptr b_ = ((sl) < 2 ? bp0[n] : bp1[n]) + ((sl) & 1) * G1_SB + (ks) * 8192; \
      B_[n] = cat8(trread(b_), trread(b_ + 1024)); } } while (0)
#define G1_MMA(A_, B_) do { _Pragma("unroll") for (int n = 0; n < 4; ++n) _Pragma("unroll") for (int m = 0; m < MT; ++m) \
    acc[n][m] = MFMA16(B_[n], A_[m], acc[n][m]); } while (0)
#define G1_ILV() do { _Pragma("unroll") for (int i_ = 0; i_ < 6; ++i_) { SGB(0x8, 1); SGB(0x100, 2); } SGB(0x8, 4 * MT - 6); } while (0)
#define G1_STAGE(sl) do { \
    G1_READ(aY, bY, sl, 1); G1_MMA(aX, bX); G1_ILV(); SB0(); \
    BARRIER(); SB0(); \
    G1_READ(aX, bX, ((sl) + 1) & 3, 0); G1_MMA(aY, bY); G1_ILV(); SB0(); } while (0)
    BARRIER();
    STAMP(3, 1);
    G1_READ(aX, bX, 0, 0);
    SB0();
#pragma unroll 1
    for (int t = 0; t < 8; ++t) { G1_STAGE(0); G1_STAGE(1); G1_STAGE(2); G1_STAGE(3); }
    STAMP(3, 3);
    __builtin_amdgcn_s_waitcnt(0xC07F);
#pragma unroll
    for (int n = 0; n < 2; ++n)
#pragma unroll
      for (int m = 0; m < MT; ++m) *(f32x4*)(lds + ((w * 2 + n) * MT + m) * 1024 + lane * 16) = acc[2 + n][m];
    BARRIER();
  }
  char* up = ws + WS_U;
#pragma unroll
  for (int n = 0; n < 2; ++n) {
    const int ft = ((pn >> 2) * 8 + (pn & 3) + 4 * wc) * 4 + (w8 >> 2) * 2 + n;
    const u32x2 bv = *(const u32x2*)(b1 + ft * 16 + g * 4);
    const float bb0 = __uint_as_float(bv[0] << 16), bb1 = __uint_as_float(bv[0] & 0xffff0000u);
    const float bb2 = __uint_as_float(bv[1] << 16), bb3 = __uint_as_float(bv[1] & 0xffff0000u);
#pragma unroll
    for (int m = 0; m < MT; ++m) {
      const int dt = dt0 + wr * MT + m;
      u32x2 v;
      v[0] = cvtpk(gelu_tanh(acc[n][m][0] + bb0), gelu_tanh(acc[n][m][1] + bb1));
      v[1] = cvtpk(gelu_tanh(acc[n][m][2] + bb2), gelu_tanh(acc[n][m][3] + bb3));
      *(u32x2*)(up + ((size_t)(dt * 144 + (ft >> 1)) * 64 + ((ft & 1) * 2 + (g >> 1)) * 16 + c16) * 16 + (g & 1) * 8) = v;
    }
  }
  STAMP(3, 4);
  asm volatile("s_waitcnt vmcnt(0)" ::: "memory");
#undef G1_ISSUE
#undef G1_WAIT
#undef G1_READ
#undef G1_MMA
#undef G1_ILV
#undef G1_STAGE
}
__global__ __launch_bounds__(512, 1) void k4_gemm1(const u16* __restrict__ w1, const u16* __restrict__ b1, const u16* __restrict__ w2,
                                                    char* __restrict__ ws) {
  __shared__ __attribute__((aligned(16))) char lds[4 * G1_SB + 256];
  const int tid = threadIdx.x, lane = tid & 63;
  const int w8 = __builtin_amdgcn_readfirstlane(tid >> 6);
  const int id = (blockIdx.x & 7) * 32 + (blockIdx.x >> 3);
  const int sid = id - (id >> 5) * 2 + ((id >> 5) * 3) / 8;
  const bool spare = (id & 31) >= 30 + ((((id >> 5) + 1) * 3) / 8 - ((id >> 5) * 3) / 8);
  if (spare) {
    const int xg = id >> 5, sp = 2 * xg - (xg * 3) / 8 + ((id & 31) - (30 + (((xg + 1) * 3) / 8 - (xg * 3) / 8)));
    const unsigned lds0 = (unsigned)(uintptr_t)lds;
    const char* p = (const char*)w2 + (size_t)(NW2 * 2048 + sp * 8 + w8) * 1024;
#pragma unroll 1
    for (int j = NW2 * 2048 + sp * 8 + w8; j < 18432; j += 104) {
      glds16(p, (unsigned)lane * 16u, lds0 + (unsigned)w8 * 1024u);
      p += 104 * 1024;
      asm volatile("s_waitcnt vmcnt(" STR_(HTHR) ")" ::: "memory");
    }
    asm volatile("s_waitcnt vmcnt(0)" ::: "memory");
    return;
  }
  const int grp = sid / 27, r = sid - grp * 27;
  STAMP(3, 0);
  asm volatile("" :: "s"(b1), "s"(ws));
  if (r < 18) g1_body<3>(lds, w1, b1, ws, lane, w8, (r % 6) * 6, grp * 4 + r / 6);
  else        g1_body<2>(lds, w1, b1, ws, lane, w8, (r - 18) * 4, grp * 4 + 3);
}

constexpr int G2_ST = 4, G2_SB = 36864;
__global__ __launch_bounds__(512, 1) void k5_gemm2(const u16* __restrict__ w2, const u16* __restrict__ b2, char* __restrict__ ws,
                                                    u16* __restrict__ out) {
  __shared__ __attribute__((aligned(16))) char lds[G2_ST * G2_SB + 256];
  const int tid = threadIdx.x, lane = tid & 63;
  const int w8 = __builtin_amdgcn_readfirstlane(tid >> 6), w = w8 & 3;
  const int xg = blockIdx.x & 7, o = blockIdx.x >> 3;
  const int kq = xg & 3, pm = o & 3, pn = (xg >> 2) * 8 + (o >> 2);
  const int tile = pn * 4 + pm;
  const unsigned lds0 = (unsigned)(uintptr_t)lds;
  STAMP(4, 0);
  if (w8 >= 4) {
    const char* ub = ws + WS_U + (size_t)(pm * 9) * 147456 + (size_t)kq * 36864;
    unsigned aoff[5];
#pragma unroll
    for (int i = 0; i < 5; ++i) { int a = w + 4 * i; if (a >= 18) a -= 4; aoff[i] = (unsigned)((a >> 1) * 147456 + (a & 1) * 1024 + lane * 16); }
    const char* wb = (const char*)w2 + (size_t)kq * 1152 * 4096 + (size_t)((pn >> 2) * 8 + (pn & 3)) * 128;
    unsigned boff[4];
#pragma unroll
    for (int i = 0; i < 4; ++i) {
      const int row = (4 * w + i) * 4 + (lane >> 4), cp = lane & 15;
      const int f = (row & 3) | (((row >> 3) & 1) << 2);
      const int ch = (((cp >> 1) ^ f) << 1) | (cp & 1);
      boff[i] = (unsigned)(row * 4096 + (ch & 7) * 16 + (ch >> 3) * 512);
    }
#define G2_ISSUE(NA, t, slot) do { const unsigned sl_ = lds0 + (unsigned)(slot) * G2_SB; \
    const char* ab_ = ub + (size_t)(t) * 2048; const char* bb_ = wb + (size_t)(t) * (64 * 4096); \
    _Pragma("unroll") for (int i = 0; i < 4; ++i) glds16(bb_, boff[i], sl_ + 20480u + (unsigned)(4 * w + i) * 1024u); \
    _Pragma("unroll") for (int i = 0; i < NA; ++i) glds16(ab_, aoff[i], sl_ + (unsigned)(w + 4 * i) * 1024u); } while (0)
#define G2_LOADER(NA, W2_, W1_) do { \
    _Pragma("unroll") for (int t = 0; t < G2_ST - 1; ++t) G2_ISSUE(NA, t, t); \
    int islot = G2_ST - 1; \
    for (int t = 0; t < 18 - (G2_ST - 1); ++t) { \
      asm volatile("s_waitcnt vmcnt(" #W2_ ")\n\ts_barrier" ::: "memory"); \
      G2_ISSUE(NA, t + G2_ST - 1, islot); \
      islot = (islot + 1 == G2_ST) ? 0 : islot + 1; } \
    asm volatile("s_waitcnt vmcnt(" #W2_ ")\n\ts_barrier" ::: "memory"); \
    _Pragma("unroll") for (int i4 = 0; i4 < 5; ++i4) {         \
      const int tau_ = ((i4 == 4 && kq >= 2) ? 3 : i4) * 4 + kq; \
      const int st_ = ((pn >> 2) * 8 + (pn & 3) + 4 * (w >> 1)) * 4 + (w & 1) * 2 + (tau_ & 1); \
      glds16(ws + WS_H + (size_t)(st_ * 36 + pm * 9 + (tau_ >> 1)) * 1024, (unsigned)lane * 16u, lds0 + 2u * G2_SB + (unsigned)(w * 5 + i4) * 1024u); } \
    asm volatile("s_waitcnt vmcnt(" #W1_ ")\n\ts_barrier" ::: "memory"); \
    asm volatile("s_waitcnt vmcnt(0)\n\ts_barrier" ::: "memory"); } while (0)
    if (w < 2) G2_LOADER(5, 18, 14); else G2_LOADER(4, 16, 13);
    return;
  }
  CODE_TOUCH(SYM_K5, SYM_END, w, lds0 + G2_ST * G2_SB);
  const int g = lane >> 4, c16 = lane & 15, q4 = (lane & 15) >> 2, p4 = lane & 3;
  const int fl = q4 | ((g & 1) << 2);
  unsigned brd[2];
#pragma unroll
  for (int n = 0; n < 2; ++n) brd[n] = (unsigned)(20480 + (8 * g + q4) * 256 + (((w * 2 + n) ^ fl) * 32) + 8 * p4);
  const unsigned ard = (unsigned)(lane * 16);
  u16 bb[5]; unsigned oo[5];
#pragma unroll
  for (int i4 = 0; i4 < 5; ++i4) {
    const int ie = (i4 == 4 && kq >= 2) ? 3 : i4;
    const int tau = ie * 4 + kq, m = tau >> 1, n = tau & 1;
    const int sp_ = ((pn >> 2) * 8 + (pn & 3) + 4 * (w >> 1)) * 64 + ((w & 1) * 2 + n) * 16 + c16, d0 = pm * 144 + m * 16 + g * 4;
    oo[i4] = (unsigned)(sp_ * 576 + d0);
    bb[i4] = b2[sp_];
  }
  f32x4 acc[18];
#pragma unroll
  for (int i = 0; i < 18; ++i) acc[i] = (f32x4){0, 0, 0, 0};
  int slot = 0;
  bf16x8 aX[9], bX[2], aY[9], bY[2];
#define G2_READ(A_, B_, ks) do { const lds_cptr sp_ = (lds_cptr)lds + slot * G2_SB; \
    _Pragma("unroll") for (int n = 0; n < 2; ++n) B_[n] = cat8(trread(sp_ + brd[n] + (ks) * 8192), trread(sp_ + brd[n] + (ks) * 8192 + 1024)); \
    _Pragma("unroll") for (int m = 0; m < 9; ++m) A_[m] = ldsr128(sp_ + ard + m * 2048 + (ks) * 1024); } while (0)
#define G2_MMA(A_, B_) do { _Pragma("unroll") for (int m = 0; m < 9; ++m) { \
    acc[m * 2] = MFMA16(A_[m], B_[0], acc[m * 2]); acc[m * 2 + 1] = MFMA16(A_[m], B_[1], acc[m * 2 + 1]); } } while (0)
#define G2_ILV() do { _Pragma("unroll") for (int i_ = 0; i_ < 13; ++i_) { SGB(0x8, 1); SGB(0x100, 1); } SGB(0x8, 5); } while (0)
  asm volatile("" :: "s"(b2), "s"(ws), "s"(out));
  BARRIER();
  STAMP(4, 1);
  __builtin_amdgcn_s_waitcnt(0xC07F);
  G2_READ(aX, bX, 0);
  SB0();
  for (int t = 0; t < 17; ++t) {
    G2_READ(aY, bY, 1); G2_MMA(aX, bX); G2_ILV(); SB0();
    BARRIER();
    slot = (slot + 1 == G2_ST) ? 0 : slot + 1;
    SB0();
    G2_READ(aX, bX, 0); G2_MMA(aY, bY); G2_ILV(); SB0();
  }
  G2_READ(aY, bY, 1); G2_MMA(aX, bX); G2_ILV(); SB0();
  G2_MMA(aY, bY);
  STAMP(4, 3);
  const auto rsrc = __builtin_amdgcn_make_buffer_rsrc(ws + WS_SLAB + (size_t)tile * SLABT, 0, SLABT, 0x00020000);
#define PKW(i, e) __builtin_elementwise_min(cvtpk(acc[i][2 * (e)], acc[i][2 * (e) + 1]), 0xFFFFFFFEu)
#pragma unroll
  for (int dst = 0; dst < 4; ++dst) {
    if (dst != kq) {
      const int sb = (dst * 4 + kq) * 10240 + w * 2560;
      __builtin_amdgcn_raw_buffer_store_b128((u32x4){PKW(dst, 0), PKW(dst, 1), PKW(dst + 4, 0), PKW(dst + 4, 1)}, rsrc, sb + lane * 16, 0, 16);
      __builtin_amdgcn_raw_buffer_store_b128((u32x4){PKW(dst + 8, 0), PKW(dst + 8, 1), PKW(dst + 12, 0), PKW(dst + 12, 1)}, rsrc, sb + 1024 + lane * 16, 0, 16);
      if (dst < 2) __builtin_amdgcn_raw_buffer_store_b64((u32x2){PKW((dst + 16) % 18, 0), PKW((dst + 16) % 18, 1)}, rsrc, sb + 2048 + lane * 8, 0, 16);
    }
  }
#undef PKW
  STAMP(4, 4);
  f32x4 own[5];
  const unsigned km0 = eqmask(kq, 0), km1 = eqmask(kq, 1), km2 = eqmask(kq, 2), km3 = eqmask(kq, 3);
#pragma unroll
  for (int i4 = 0; i4 < 5; ++i4) {
#pragma unroll
    for (int j = 0; j < 4; ++j)
      own[i4][j] = sel4(acc[i4 * 4][j], acc[i4 * 4 + 1][j], acc[(i4 * 4 + 2) % 18][j], acc[(i4 * 4 + 3) % 18][j], km0, km1, km2, km3);
    own[i4] += *(const f32x4*)(lds + 2 * G2_SB + (w * 5 + i4) * 1024 + lane * 16) + bf2f(bb[i4]);
  }
  asm volatile("s_waitcnt vmcnt(0)" ::: "memory");
  u32x4 pa[3], pb[3]; u32x2 pc[3];
  const int x8 = (kq < 2) ? 2048 + lane * 8 : lane * 16;
  for (unsigned spins = 0;; ++spins) {
#pragma unroll
    for (int s = 0; s < 3; ++s) {
      const int sb = (kq * 4 + ((kq + s + 1) & 3)) * 10240 + w * 2560;
      pa[s] = __builtin_amdgcn_raw_buffer_load_b128(rsrc, sb + lane * 16, 0, 16);
      pb[s] = __builtin_amdgcn_raw_buffer_load_b128(rsrc, sb + 1024 + lane * 16, 0, 16);
      pc[s] = __builtin_amdgcn_raw_buffer_load_b64(rsrc, sb + x8, 0, 16);
    }
    unsigned mx = 0;
#pragma unroll
    for (int s = 0; s < 3; ++s) {
#pragma unroll
      for (int e = 0; e < 4; ++e) mx = max(mx, max(pa[s][e], pb[s][e]));
      mx = max(mx, max(pc[s][0], pc[s][1]));
    }
    if (__all(mx != 0xFFFFFFFFu) || spins > (1u << 20)) break;
    __builtin_amdgcn_s_sleep(2);
  }
  STAMP(4, 5);
#define UPK(d) (f32x4){__uint_as_float((d)[0] << 16), __uint_as_float((d)[0] & 0xffff0000u), __uint_as_float((d)[1] << 16), __uint_as_float((d)[1] & 0xffff0000u)}
#pragma unroll
  for (int i4 = 0; i4 < 5; ++i4) {
    f32x4 a = own[i4];
#pragma unroll
    for (int s = 0; s < 3; ++s) {
      const u32x2 d = (i4 == 0) ? (u32x2){pa[s][0], pa[s][1]} : (i4 == 1) ? (u32x2){pa[s][2], pa[s][3]} :
                      (i4 == 2) ? (u32x2){pb[s][0], pb[s][1]} : (i4 == 3) ? (u32x2){pb[s][2], pb[s][3]} : pc[s];
      a += UPK(d);
    }
    if (i4 < 4 || kq < 2) *(u32x2*)(out + oo[i4]) = (u32x2){cvtpk(a[0], a[1]), cvtpk(a[2], a[3])};
  }
#undef UPK
  STAMP(4, 6);
#undef G2_ISSUE
#undef G2_READ
#undef G2_MMA
#undef G2_LOADER
}

__global__ void k_end() {}

extern "C" void kernel_launch(void* const* d_in, const int* in_sizes, int n_in,
                              void* d_out, int out_size, void* d_ws, size_t ws_size,
                              hipStream_t stream) {
  const u16* x    = (const u16*)d_in[0];
  const u16* wqkv = (const u16*)d_in[1];
  const u16* wo   = (const u16*)d_in[2];
  const u16* w1   = (const u16*)d_in[4];
  const u16* b1   = (const u16*)d_in[5];
  const u16* w2   = (const u16*)d_in[6];
  const u16* b2   = (const u16*)d_in[7];
  char* ws = (char*)d_ws;
  k1_qkv<<<256, 512, 0, stream>>>(x, wqkv, wo, ws);
  k2_attn<<<256, 512, 0, stream>>>(ws, w1, w2);
  k3_proj<<<256, 576, 0, stream>>>(x, ws);
  k4_gemm1<<<256, 512, 0, stream>>>(w1, b1, w2, ws);
  k5_gemm2<<<256, 512, 0, stream>>>(w2, b2, ws, (u16*)d_out);
}
```

```cpp
#include <hip/hip_runtime.h>
#include <stdint.h>
#define OUT_PLAIN

typedef short    bf16x8 __attribute__((ext_vector_type(8)));
typedef short    s16x4  __attribute__((ext_vector_type(4)));
typedef float    f32x4  __attribute__((ext_vector_type(4)));
typedef float    f32x16 __attribute__((ext_vector_type(16)));
typedef unsigned u32x2  __attribute__((ext_vector_type(2)));
typedef unsigned u32x4  __attribute__((ext_vector_type(4)));
typedef __bf16   bf16x2_t __attribute__((ext_vector_type(2)));
typedef unsigned short u16;
#define LDS_AS __attribute__((address_space(3)))
#define GLB_AS __attribute__((address_space(1)))
typedef LDS_AS const char* lds_cptr;
typedef GLB_AS unsigned gu32;
typedef GLB_AS unsigned long long gu64;
#define RLX_AGENT __ATOMIC_RELAXED, __HIP_MEMORY_SCOPE_AGENT

constexpr int S_ = 2048, D_ = 576, F_ = 4608;
constexpr float LN_EPS = 1e-5f;
constexpr float QSCALE = 0.23570226039551584f * 1.4426950408889634f;

constexpr unsigned VREC    = 640;
constexpr unsigned KXOFF   = 65536 + 128 * VREC;
constexpr unsigned KVHEAD  = KXOFF + 8192;
constexpr size_t WS_QM   = 0;
constexpr size_t WS_QX   = WS_QM + 32u * 65536;
constexpr size_t WS_KV   = WS_QX + 32u * 8192;
constexpr size_t WS_OF   = WS_KV + 32u * KVHEAD;
constexpr size_t WS_WOF  = WS_OF + 128u * 18 * 1024;
constexpr size_t WS_HN   = WS_WOF + 36u * 18 * 1024;
constexpr size_t WS_H    = WS_HN + 36u * 64 * 1024;
constexpr size_t WS_U    = WS_H + 2048u * 576 * 4;
constexpr size_t WS_GRAN = WS_U + 36u * 144 * 1024;
constexpr size_t WS_SLAB = WS_GRAN + 64u * 4 * 512;
constexpr unsigned SLABT = 16u * 10240;

__device__ __forceinline__ unsigned cvtpk(float lo, float hi) {
  typedef float f32x2_t __attribute__((ext_vector_type(2)));
  return __builtin_bit_cast(unsigned, __builtin_convertvector((f32x2_t){lo, hi}, bf16x2_t));
}
__device__ __forceinline__ float bf2f(u16 v) { return __uint_as_float((unsigned)v << 16); }
__device__ __forceinline__ void glds16(const void* sbase, unsigned voff, unsigned lds_base) {
  unsigned sv;
  asm volatile("s_mov_b32 %0, m0\n\ts_mov_b32 m0, %3\n\ts_nop 0\n\tglobal_load_lds_dwordx4 %1, %2\n\ts_mov_b32 m0, %0"
               : "=&s"(sv) : "v"(voff), "s"(sbase), "s"(lds_base) : "memory");
}
__device__ __forceinline__ void touch4(const void* sbase, unsigned voff, unsigned lds_base) {
  unsigned sv;
  asm volatile("s_mov_b32 %0, m0\n\ts_mov_b32 m0, %3\n\ts_nop 0\n\tglobal_load_lds_dword %1, %2\n\ts_mov_b32 m0, %0"
               : "=&s"(sv) : "v"(voff), "s"(sbase), "s"(lds_base) : "memory");
}
__device__ __forceinline__ s16x4 trread(lds_cptr p) {
  typedef short v4i16_t __attribute__((ext_vector_type(4)));
  return __builtin_bit_cast(s16x4, __builtin_amdgcn_ds_read_tr16_b64_v4i16((LDS_AS v4i16_t*)p));
}
__device__ __forceinline__ bf16x8 cat8(s16x4 a, s16x4 b) {
  return (bf16x8){a[0], a[1], a[2], a[3], b[0], b[1], b[2], b[3]};
}
__device__ __forceinline__ bf16x8 ldsr128(lds_cptr p) { return *(const LDS_AS bf16x8*)p; }
#define MFMA16(a, b, c) __builtin_amdgcn_mfma_f32_16x16x32_bf16((a), (b), (c), 0, 0, 0)
#define MFMA32(a, b, c) __builtin_amdgcn_mfma_f32_32x32x16_bf16((a), (b), (c), 0, 0, 0)
#define SB0() __builtin_amdgcn_sched_barrier(0)
#define SGB(mask, n) __builtin_amdgcn_sched_group_barrier((mask), (n), 0)
#define WAITV(n) asm volatile("s_waitcnt vmcnt(" #n ")" ::: "memory")
#define BARRIER() do { asm volatile("" ::: "memory"); __builtin_amdgcn_s_waitcnt(0xC07F); __builtin_amdgcn_s_barrier(); asm volatile("" ::: "memory"); } while (0)
__device__ __forceinline__ float dot2(unsigned a, unsigned b, float c) {
  return __builtin_amdgcn_fdot2_f32_bf16(__builtin_bit_cast(bf16x2_t, a), __builtin_bit_cast(bf16x2_t, b), c, false);
}
constexpr unsigned ONES2 = 0x3F803F80u;
__device__ __forceinline__ void st16(void* p, u32x4 v) { asm volatile("global_store_dwordx4 %0, %1, off sc1\n\ts_nop 1" :: "v"(p), "v"(v) : "memory"); }
__device__ __forceinline__ void st8(void* p, u32x2 v) { asm volatile("global_store_dwordx2 %0, %1, off sc1" :: "v"(p), "v"(v) : "memory"); }
__device__ __forceinline__ void st4(void* p, unsigned v) { asm volatile("global_store_dword %0, %1, off sc1" :: "v"(p), "v"(v) : "memory"); }
__device__ __forceinline__ float sel4(float a, float b, float c, float d, unsigned m0, unsigned m1, unsigned m2, unsigned m3) {
  return __uint_as_float((__float_as_uint(a) & m0) | (__float_as_uint(b) & m1) | (__float_as_uint(c) & m2) | (__float_as_uint(d) & m3));
}
__device__ __forceinline__ unsigned eqmask(int a, int b) { unsigned m = (a == b) ? ~0u : 0u; asm volatile("" : "+v"(m)); return m; }
#define STAMP(kid, i) do {} while (0)
#define TSTAMP(i) do {} while (0)
#define TFLUSH(kid, a, b) do {} while (0)

#define CODE_ADDR(dst, sym) do { unsigned lo_, hi_; \
    asm volatile("s_getpc_b64 s[98:99]\n\ts_add_u32 %0, s98, " sym "@rel32@lo+4\n\ts_addc_u32 %1, s99, " sym "@rel32@hi+12" \
                 : "=s"(lo_), "=s"(hi_) :: "s98", "s99", "scc"); \
    dst = (const char*)(((unsigned long long)hi_ << 32) | lo_); } while (0)

#define CODE_TOUCH(symA, symB, slot, ldsd) do { const char* ca_; const char* cb_; CODE_ADDR(ca_, symA); CODE_ADDR(cb_, symB); \
    const unsigned ns_ = (unsigned)(cb_ - ca_) >> 6; \
    for (unsigned c0_ = (unsigned)(slot) * 64u; c0_ < ns_; c0_ += 512u) { const unsigned ci_ = c0_ + lane; touch4(ca_, (ci_ < ns_ ? ci_ : 0u) * 64u, (ldsd)); } } while (0)
#define SYM_K1 "_Z6k1_qkvPKtS0_S0_Pc"
#define SYM_K2 "_Z7k2_attnPcPKtS1_"
#define SYM_K3 "_Z7k3_projPKtPc"
#define SYM_K4 "_Z8k4_gemm1PKtS0_S0_Pc"
#define SYM_K5 "_Z8k5_gemm2PKtS0_PcPt"
#define SYM_END "_Z5k_endv"

constexpr int Q_ST = 5, Q_SB = 30720;
__global__ __launch_bounds__(512, 1) void k1_qkv(const u16* __restrict__ x, const u16* __restrict__ wqkv, const u16* __restrict__ wo,
                                                  char* __restrict__ ws) {
  __shared__ __attribute__((aligned(16))) char lds[Q_ST * Q_SB + 512 + 1024];
  const int tid = threadIdx.x, lane = tid & 63;
  const int w8 = __builtin_amdgcn_readfirstlane(tid >> 6), w = w8 & 3;
  const int b = blockIdx.x, xg = b & 7, o = b >> 3;
  const int bm = (xg >> 1) * 4 + (o & 3), hg = (xg & 1) * 8 + (o >> 2);
  const unsigned lds0 = (unsigned)(uintptr_t)lds;
  float* csl = (float*)(lds + Q_ST * Q_SB);
  constexpr unsigned DUMMY = Q_ST * Q_SB + 512;
  constexpr int SP = 304;
  u16 wog[8];
  STAMP(0, 0);
  if (w8 >= 4) {
    const char* xb = (const char*)x + (size_t)bm * 128 * 1152;
    const char* wb = (const char*)wqkv + (216 * hg - 8 * (hg & 1));
    unsigned xoff[4], woff[4];
#pragma unroll
    for (int i = 0; i < 4; ++i) {
      const int row = (w + 4 * i) * 8 + (lane >> 3);
      xoff[i] = (unsigned)(row * 1152 + (((lane & 7) ^ (lane >> 3)) * 16));
      int pc = w + 4 * i; if (pc >= 14) pc -= 4;
      const unsigned c = (unsigned)(pc * 64 + lane), pos = c / 14u, ch = c - pos * 14u;
      const unsigned k = (pos & ~12u) | ((pos & 4u) << 1) | ((pos & 8u) >> 1);
      woff[i] = k * 3456u + ch * 16u;
    }
    int it_ = 0; unsigned isl = lds0;
#define Q_ISSUE() do { const char* xb_ = xb + (size_t)it_ * 128; const char* wb_ = wb + (size_t)it_ * (64 * 3456); \
    _Pragma("unroll") for (int i = 0; i < 3; ++i) glds16(wb_, woff[i], isl + 16384u + (unsigned)(w + 4 * i) * 1024u); \
    if (w < 2) glds16(wb_, woff[3], isl + 16384u + (unsigned)(w + 12) * 1024u); \
    _Pragma("unroll") for (int i = 0; i < 4; ++i) glds16(xb_, xoff[i], isl + (unsigned)(w + 4 * i) * 1024u); \
    ++it_; isl = (isl + Q_SB == lds0 + Q_ST * Q_SB) ? lds0 : isl + Q_SB; } while (0)
#define Q_WAIT(NA, NB) do { if (w < 2) asm volatile("s_waitcnt vmcnt(" #NA ")\n\ts_barrier" ::: "memory"); \
                            else       asm volatile("s_waitcnt vmcnt(" #NB ")\n\ts_barrier" ::: "memory"); } while (0)
    Q_ISSUE(); Q_ISSUE(); Q_ISSUE();
    Q_WAIT(16, 14); Q_ISSUE(); Q_ISSUE();
#pragma unroll 1
    for (int t = 1; t < 5; ++t) { Q_WAIT(24, 21); Q_ISSUE(); }
    {
      const int tk = b * 162 + ((tid - 256 < 162) ? tid - 256 : 0);
      const int l_ = tk & 63, ks = (tk >> 6) % 18, nt = tk / (64 * 18);
      const int g_ = l_ >> 4, n_ = nt * 16 + (l_ & 15);
      const int k0 = (ks < 16) ? 18 * (2 * ks + (g_ >> 1)) + 4 * (g_ & 1) : 18 * (16 * (ks - 16) + 4 * g_) + 16;
      const u16* wp_ = wo + k0 * 576 + n_;
#pragma unroll
      for (int j = 0; j < 8; ++j) {
        const int dk = (ks < 16) ? (j & 3) + 8 * (j >> 2) : 18 * (j >> 1) + (j & 1);
        wog[j] = wp_[dk * 576];
      }
    }
    Q_WAIT(32, 29);
    Q_WAIT(24, 22);
    Q_WAIT(16, 15);
    Q_WAIT(8, 8);
    if (tid - 256 < 32) st16(ws + WS_GRAN + (size_t)b * 512 + (tid - 256) * 16, (u32x4){0, 0, 0, 0});
    BARRIER();
    BARRIER();
  } else {
    const unsigned vw = (unsigned)(o * 4 + w);
    CODE_TOUCH(SYM_K1, SYM_K3, vw & 7u, lds0 + DUMMY);
    const int g = lane >> 4, c16 = lane & 15, q4 = (lane & 15) >> 2, p4 = lane & 3;
#define Q_TILE(i) ((w == 3) ? ((i) == 0 ? 6 : (i) - 1) : ((i) < 2 ? 2 * w + (i) : ((i) < 2 + 2 * w ? (i) - 2 : (i))))
    unsigned wrd[7], so[7][2]; float qs[7][2];
#pragma unroll
    for (int i = 0; i < 7; ++i) {
      const int n = Q_TILE(i);
      int Q = 4 * n + p4; if (Q > 26) Q = 26;
      wrd[i] = (unsigned)(16384 + ((g >> 1) * 16 + (g & 1) * 4 + q4) * 224 + 8 * Q + 8 * (hg & 1));
#pragma unroll
      for (int e = 0; e < 2; ++e) {
        const unsigned col = (unsigned)(n * 16 + g * 4 + e * 2);
        const unsigned hl = col / 18u;
        so[i][e] = (unsigned)((w * 32 + c16) * SP) + col * 2u + hl * 12u;
        qs[i][e] = ((unsigned)hg * 6u + hl < 32u) ? QSCALE : 1.0f;
        asm volatile("" : "+v"(so[i][e]), "+v"(qs[i][e]));
      }
    }
    const unsigned xrd0 = (unsigned)((w * 32 + c16) * 128 + ((g ^ (c16 & 7)) * 16)), xrd1 = xrd0 ^ 64u;
    f32x4 acc[7][2], csa[2], sq[2], sx[2];
#pragma unroll
    for (int n = 0; n < 7; ++n) { acc[n][0] = (f32x4){0, 0, 0, 0}; acc[n][1] = (f32x4){0, 0, 0, 0}; }
#pragma unroll
    for (int i = 0; i < 2; ++i) { csa[i] = (f32x4){0, 0, 0, 0}; sq[i] = (f32x4){0, 0, 0, 0}; sx[i] = (f32x4){0, 0, 0, 0}; }
    const bf16x8 ones8 = __builtin_bit_cast(bf16x8, (u32x4){ONES2, ONES2, ONES2, ONES2});
    bf16x8 wX[7], xX[2], wY[7], xY[2];
    int slot = 0;
#define Q_READ(W_, X_, ks) do { const lds_cptr sp_ = (lds_cptr)lds + slot * Q_SB; \
    X_[0] = ldsr128(sp_ + ((ks) ? xrd1 : xrd0)); X_[1] = ldsr128(sp_ + ((ks) ? xrd1 : xrd0) + 2048); \
    _Pragma("unroll") for (int n = 0; n < 7; ++n) W_[n] = cat8(trread(sp_ + wrd[n] + (ks) * 7168), trread(sp_ + wrd[n] + (ks) * 7168 + 1792)); } while (0)
#define Q_MMA(W_, X_) do { \
    sx[0] = MFMA16(ones8, X_[0], sx[0]); sx[1] = MFMA16(ones8, X_[1], sx[1]); \
    sq[0] = MFMA16(X_[0], X_[0], sq[0]); sq[1] = MFMA16(X_[1], X_[1], sq[1]); \
    _Pragma("unroll") for (int n = 0; n < 7; ++n) { acc[n][0] = MFMA16(W_[n], X_[0], acc[n][0]); acc[n][1] = MFMA16(W_[n], X_[1], acc[n][1]); \
      if (n < 2) csa[n] = MFMA16(W_[n], ones8, csa[n]); } } while (0)
#define Q_ILV() do { _Pragma("unroll") for (int i_ = 0; i_ < 16; ++i_) { SGB(0x8, 1); SGB(0x100, 1); } SGB(0x8, 4); } while (0)
#define TDELAY 16
    {
      __builtin_amdgcn_s_sleep(TDELAY);
      unsigned i = vw * 64u + lane, rw = i / 27u;
      const char* wq = (const char*)wqkv + 192 * 3456 + (xg & 1) * 1728;
      touch4(wq, rw * 3456u + (i - rw * 27u) * 64u, lds0 + DUMMY);
      if (vw < 96u) { rw = i / 12u; touch4((const char*)x + (size_t)(xg >> 1) * 589824 + 384, rw * 1152u + (i - rw * 12u) * 64u, lds0 + DUMMY); }
      else { i += 2048u; rw = i / 27u; touch4(wq, rw * 3456u + (i - rw * 27u) * 64u, lds0 + DUMMY); }
      if (vw < 2u) { i = 10240u + vw * 64u + lane; rw = i / 27u; touch4(wq, rw * 3456u + (i - rw * 27u) * 64u, lds0 + DUMMY); }
    }
    asm volatile("" :: "s"(ws));
    BARRIER();
    STAMP(0, 1);
    __builtin_amdgcn_s_waitcnt(0xC07F);
    Q_READ(wX, xX, 0);
    SB0();
#pragma unroll
    for (int t = 0; t < 9; ++t) {
      Q_READ(wY, xY, 1); Q_MMA(wX, xX); Q_ILV(); SB0();
      if (t < 8) {
        BARRIER(); slot = (slot + 1 == Q_ST) ? 0 : slot + 1;
        SB0();
        Q_READ(wX, xX, 0); Q_MMA(wY, xY); Q_ILV(); SB0();
      } else { Q_MMA(wY, xY); }
    }
    STAMP(0, 2);
    if (c16 == 0) { *(f32x4*)(csl + (2 * w) * 16 + g * 4) = csa[0]; if (w < 3) *(f32x4*)(csl + (2 * w + 1) * 16 + g * 4) = csa[1]; }
    float rstd[2], mr[2];
    const unsigned jm0 = eqmask(c16 & 3, 0), jm1 = eqmask(c16 & 3, 1), jm2 = eqmask(c16 & 3, 2), jm3 = eqmask(c16 & 3, 3);
#pragma unroll
    for (int tt = 0; tt < 2; ++tt) {
      const float dg = sel4(sq[tt][0], sq[tt][1], sq[tt][2], sq[tt][3], jm0, jm1, jm2, jm3);
      const float ss = __shfl(dg, (c16 >> 2) * 16 + c16);
      const float mean = sx[tt][0] * (1.f / 576.f);
      rstd[tt] = rsqrtf(fmaxf(ss * (1.f / 576.f) - mean * mean, 0.f) + LN_EPS);
      mr[tt] = -mean;
    }
    BARRIER();
#pragma unroll
    for (int n = 0; n < 7; ++n) {
      const int nt = Q_TILE(n);
      const f32x4 cs4 = *(const f32x4*)(csl + nt * 16 + g * 4);
#pragma unroll
      for (int e = 0; e < 2; ++e)
#pragma unroll
        for (int tt = 0; tt < 2; ++tt) {
          const float rs = rstd[tt] * qs[n][e];
          const float v0 = __builtin_fmaf(mr[tt], cs4[2 * e], acc[n][tt][2 * e]) * rs;
          const float v1 = __builtin_fmaf(mr[tt], cs4[2 * e + 1], acc[n][tt][2 * e + 1]) * rs;
          *(unsigned*)(lds + so[n][e] + tt * 16 * SP) = cvtpk(v0, v1);
        }
    }
    BARRIER();
  }
  STAMP(0, 3);
  {
    const unsigned t8 = (unsigned)tid & 255u;
    const unsigned Tl = t8 >> 1, c = t8 & 1u, T = (unsigned)bm * 128u + Tl;
    const unsigned oQK = (T >> 5) * 1024u + c * 512u + (T & 31u) * 16u;
    const unsigned T2 = (unsigned)bm * 128u + (t8 & 127u);
    const unsigned oKX = KXOFF + (T2 >> 7) * 512u + (T2 & 31u) * 16u + ((T2 >> 5) & 3u) * 4u;
    const unsigned vrec = t8 >> 5, vhh = (t8 >> 4) & 1u, vi = t8 & 15u;
    const lds_cptr vsrc = (lds_cptr)lds + ((vrec * 16u + vhh * 4u + (vi >> 2)) * SP + (vi & 3u) * 8u);
    const lds_cptr vsrx = (lds_cptr)lds + ((vrec * 16u + vhh * 4u + (vi >> 2)) * SP + 32u);
    const unsigned oV = 65536u + ((unsigned)bm * 8u + vrec) * VREC + vhh * 320u + vi * 16u;
#pragma unroll
    for (int i = 0; i < 3; ++i) {
      const int hl = (w8 >> 2) * 3 + i;
      const int hs = hg * 6 + hl, which = hs >> 5, h = hs & 31;
      char* base = ws + (which == 0 ? WS_QM + (size_t)h * 65536 : WS_KV + (size_t)h * KVHEAD);
      if (which == 2) {
        const bf16x8 vm = cat8(trread(vsrc + hl * 48), trread(vsrc + hl * 48 + 8 * SP));
        bf16x8 vx = cat8(trread(vsrx + hl * 48), trread(vsrx + hl * 48 + 8 * SP));
        st16(base + oV, __builtin_bit_cast(u32x4, vm));
        if (vi == 2u) vx = __builtin_bit_cast(bf16x8, (u32x4){ONES2, ONES2, ONES2, ONES2});
        if (vi == 3u) vx = (bf16x8){0, 0, 0, 0, 0, 0, 0, 0};
        if (vi < 4u) st16(base + oV + 256u, __builtin_bit_cast(u32x4, vx));
      } else {
        st16(base + oQK, *(const u32x4*)(lds + Tl * SP + hl * 48 + c * 16));
        if (t8 < 128u) {
          const unsigned xv = *(const unsigned*)(lds + t8 * SP + hl * 48 + 32);
          if (which == 0) st4(ws + WS_QX + (size_t)h * 8192 + T2 * 4u, xv);
          else st4(base + oKX, xv);
        }
      }
    }
  }
  if (tid >= 256 && tid - 256 < 162) {
    u32x4 v;
#pragma unroll
    for (int j = 0; j < 4; ++j) v[j] = (unsigned)wog[2 * j] | ((unsigned)wog[2 * j + 1] << 16);
    st16(ws + WS_WOF + (size_t)(b * 162 + tid - 256) * 16, v);
  }
  STAMP(0, 4);
  asm volatile("s_waitcnt vmcnt(0)" ::: "memory");
#undef Q_ISSUE
#undef Q_TILE
#undef Q_WAIT
#undef Q_READ
#undef Q_MMA
#undef Q_ILV
}

#define NW2 7
#define HTHR 6
#define STR2_(x) #x
#define STR_(x) STR2_(x)
#define EX2(v) __builtin_amdgcn_exp2f(v)
__global__ __launch_bounds__(512, 1) void k2_attn(char* __restrict__ ws, const u16* __restrict__ w1, const u16* __restrict__ w2) {
  __shared__ __attribute__((aligned(16))) char lds[KVHEAD + 1024];
  const int tid = threadIdx.x, lane = tid & 63;
  const int w = __builtin_amdgcn_readfirstlane(tid >> 6);
  const int b = blockIdx.x, xg = b & 7;
  const int h = xg * 4 + ((b >> 3) & 3), qt = b >> 5;
  const int qp = w & 3, half = w >> 2;
  const unsigned lds0 = (unsigned)(uintptr_t)lds;
  const int r = lane & 31, hh = lane >> 5;
  const char* kvg = ws + WS_KV + (size_t)h * KVHEAD;
  const unsigned l16 = (unsigned)lane * 16u;
  STAMP(1, 0);
  u32x4 qmA, qmB; unsigned qxA, qxB;
  {
    const char* qmp = ws + WS_QM + (size_t)h * 65536 + (size_t)(qt * 8 + qp * 2) * 1024 + lane * 16;
    const char* qxp = ws + WS_QX + (size_t)h * 8192 + (size_t)(qt * 256 + qp * 64 + r) * 4;
    asm volatile("global_load_dwordx4 %0, %1, off" : "=v"(qmA) : "v"(qmp) : "memory");
    asm volatile("global_load_dwordx4 %0, %1, off offset:1024" : "=v"(qmB) : "v"(qmp) : "memory");
    asm volatile("global_load_dword %0, %1, off" : "=v"(qxA) : "v"(qxp) : "memory");
    asm volatile("global_load_dword %0, %1, off offset:128" : "=v"(qxB) : "v"(qxp) : "memory");
  }
#pragma unroll
  for (int i = 0; i < 6; ++i) {
    const int s = i * 8 + w;
    const int p = (s < 10) ? s : (s < 20) ? 32 + s - 10 : (s < 30) ? 64 + s - 20 : (s < 40) ? 104 + s - 30 : 144 + s - 40;
    glds16(kvg + (size_t)p * 1024, l16, lds0 + p * 1024);
  }
#define STAGEB(i) do { const int s_ = (i) * 8 + w; \
    const int p_ = (s_ < 22) ? 10 + s_ : (s_ < 44) ? 42 + s_ - 22 : (s_ < 74) ? 74 + s_ - 44 : 114 + s_ - 74; \
    glds16(kvg + (size_t)p_ * 1024, l16, lds0 + p_ * 1024); } while (0)
  CODE_TOUCH(SYM_K2, SYM_K3, w, lds0 + KVHEAD);
  const lds_cptr vp = (lds_cptr)lds + (65536u + (unsigned)half * 64u * VREC + hh * 320 + (r < 19 ? r : 19) * 16);
  const lds_cptr kp = (lds_cptr)lds + (half * 32768 + hh * 512 + r * 16);
  const lds_cptr xp = (lds_cptr)lds + (KXOFF + half * 4096 + r * 16);

  const char* pfb = (const char*)w1 + (size_t)(b * 8 + w) * 1024;
  asm volatile("s_waitcnt vmcnt(0)\n\ts_barrier" : "+v"(qmA), "+v"(qmB), "+v"(qxA), "+v"(qxB) :: "memory");
  STAMP(1, 1);
  const bf16x8 qA = __builtin_bit_cast(bf16x8, qmA), qB = __builtin_bit_cast(bf16x8, qmB);
  bf16x8 eA[4], eB[4];
  {
    const unsigned a = hh == 0 ? qxA : 0u, bq = hh == 0 ? qxB : 0u;
    eA[0] = __builtin_bit_cast(bf16x8, (u32x4){a, 0u, 0u, 0u}); eB[0] = __builtin_bit_cast(bf16x8, (u32x4){bq, 0u, 0u, 0u});
    eA[1] = __builtin_bit_cast(bf16x8, (u32x4){0u, a, 0u, 0u}); eB[1] = __builtin_bit_cast(bf16x8, (u32x4){0u, bq, 0u, 0u});
    eA[2] = __builtin_bit_cast(bf16x8, (u32x4){0u, 0u, a, 0u}); eB[2] = __builtin_bit_cast(bf16x8, (u32x4){0u, 0u, bq, 0u});
    eA[3] = __builtin_bit_cast(bf16x8, (u32x4){0u, 0u, 0u, a}); eB[3] = __builtin_bit_cast(bf16x8, (u32x4){0u, 0u, 0u, bq});
  }
  const f32x16 zero16 = {};
  f32x16 oA = {}, oB = {}, sA, sB;
  u32x4 pAl = {0u, 0u, 0u, 0u}, pAh = {0u, 0u, 0u, 0u}, pBl = {0u, 0u, 0u, 0u}, pBh = {0u, 0u, 0u, 0u};
  bf16x8 kf, kx, kxn, v0, v1 = {};
  kf = ldsr128(kp); kx = ldsr128(xp);
  sA = MFMA32(kf, qA, zero16); sA = MFMA32(kx, eA[0], sA);
  float e0 = 0.f, e1 = 0.f, e2 = 0.f, e3 = 0.f, f0, f1, f2, f3;
  SB0();
#define EX4(a, b_, c_, d_, S, i0) do { a = EX2(S[i0]); b_ = EX2(S[i0 + 1]); c_ = EX2(S[i0 + 2]); d_ = EX2(S[i0 + 3]); } while (0)
#define CV2(P, j0, a, b_, c_, d_) do { P[j0] = cvtpk(a, b_); P[j0 + 1] = cvtpk(c_, d_); } while (0)
#define PF8(x) __builtin_bit_cast(bf16x8, x)
#define PIN(x) asm volatile("" : "+v"(x))
#define PINE() asm volatile("" : "+v"(e0), "+v"(e1), "+v"(e2), "+v"(e3))
#define PINF() asm volatile("" : "+v"(f0), "+v"(f1), "+v"(f2), "+v"(f3))
#define EXA(a, b_, S, i0) do { a = EX2(S[i0]); b_ = EX2(S[i0 + 1]); asm volatile("" : "+v"(a), "+v"(b_)); SB0(); } while (0)
#define EXB(c_, d_, S, i0) do { c_ = EX2(S[i0 + 2]); d_ = EX2(S[i0 + 3]); } while (0)
#define TILE(t) do { \
    constexpr int ro_ = (t) * 2 * (int)VREC; \
      sB = MFMA32(kf, qB, zero16); PIN(sB); SB0(); v0 = ldsr128(vq + ro_); kf = ldsr128(kq + ((t) + 1) * 1024); \
           if ((t) == 3) { kxn = ldsr128(xq + 512); } \
           EX4(f0, f1, f2, f3, sA, 0); CV2(pBh, 2, e0, e1, e2, e3); PINF(); PIN(pBh); SB0(); \
      EXA(e0, e1, sA, 4); oB = MFMA32(v1, PF8(pBh), oB); PIN(oB); SB0(); v1 = ldsr128(vq + ro_ + VREC); \
           EXB(e2, e3, sA, 4); CV2(pAl, 0, f0, f1, f2, f3); PINE(); PIN(pAl); SB0(); \
      EXA(f0, f1, sA, 8); sB = MFMA32(kx, eB[t], sB); PIN(sB); SB0(); \
           EXB(f2, f3, sA, 8); CV2(pAl, 2, e0, e1, e2, e3); PINF(); PIN(pAl); SB0(); \
      EXA(e0, e1, sA, 12); __builtin_amdgcn_s_waitcnt(0xC07F); SB0(); oA = MFMA32(v0, PF8(pAl), oA); PIN(oA); SB0(); \
           EXB(e2, e3, sA, 12); CV2(pAh, 0, f0, f1, f2, f3); PINE(); PIN(pAh); SB0(); \
      EXA(f0, f1, sB, 0); sA = MFMA32(kf, qA, zero16); PIN(sA); SB0(); EXB(f2, f3, sB, 0); CV2(pAh, 2, e0, e1, e2, e3); PINF(); PIN(pAh); SB0(); \
      EXA(e0, e1, sB, 4); oA = MFMA32(v1, PF8(pAh), oA); PIN(oA); SB0(); EXB(e2, e3, sB, 4); CV2(pBl, 0, f0, f1, f2, f3); PINE(); PIN(pBl); SB0(); \
      EXA(f0, f1, sB, 8); sA = MFMA32((t) == 3 ? kxn : kx, eA[((t) + 1) & 3], sA); PIN(sA); SB0(); EXB(f2, f3, sB, 8); CV2(pBl, 2, e0, e1, e2, e3); PINF(); PIN(pBl); SB0(); \
      EXA(e0, e1, sB, 12); oB = MFMA32(v0, PF8(pBl), oB); PIN(oB); SB0(); EXB(e2, e3, sB, 12); CV2(pBh, 0, f0, f1, f2, f3); PINE(); PIN(pBh); SB0(); \
  } while (0)
#define SIDE(a) do { STAGEB(it * 8 + (a)); if (it * 8 + (a) + 1 < 13) STAGEB(it * 8 + (a) + 1); SB0(); } while (0)
#define PFETCH() do { glds16(pfb, l16, lds0 + KVHEAD); pfb += 2048 * 1024; if (++pfn == 9) pfb = (const char*)w2 + (size_t)(b * 8 + w) * 1024; SB0(); } while (0)
  int pfn = 0;
  lds_cptr vq = vp, kq = kp, xq = xp;
  const auto slabr = __builtin_amdgcn_make_buffer_rsrc(ws + WS_SLAB + (size_t)b * 40960, 0, 40960, 0x00020000);
  __builtin_amdgcn_s_setprio(3);
#pragma unroll 1
  for (int it = 0; it < 2; ++it) {
    SIDE(0); TILE(0);
    SIDE(2); TILE(1);
    SIDE(4); TILE(2);
    if (it == 0) SIDE(6);
    TILE(3);
    kx = kxn;
    vq += 8 * VREC; kq += 4096; xq += 512;
  }
  asm volatile("s_waitcnt vmcnt(0)\n\ts_barrier" ::: "memory");
  STAMP(1, 2);
  if (b < 8) { CODE_TOUCH(SYM_K3, SYM_END, w, lds0 + KVHEAD); }
#pragma unroll 1
  for (int it = 2; it < 8; ++it) {
    if (it == 4) __builtin_amdgcn_s_setprio(2);
    if (it == 6) __builtin_amdgcn_s_setprio(1);
    if (it == 7) __builtin_amdgcn_s_setprio(0);
    SB0();
    PFETCH(); TILE(0);
    if (it >= 11 - NW2) { PFETCH(); }
    TILE(1);
    PFETCH();
    TILE(2);
    if (it < 7) {
      __builtin_amdgcn_raw_buffer_store_b128((u32x4){~0u, ~0u, ~0u, ~0u}, slabr, (w * 5 + it - 2) * 1024 + lane * 16, 0, 16);
      SB0();
    }
    TILE(3);
    kx = kxn;
    vq += 8 * VREC; kq += 4096; xq += 512;
  }
  CV2(pBh, 2, e0, e1, e2, e3);
  oB = MFMA32(v1, PF8(pBh), oB);
  STAMP(1, 3);

  float mA = 0.f, mB = 0.f;
  {
    bool bad = false;
    if (hh == 0) {
      bad = !(oA[10] >= 8.271806125530277e-25f && oA[10] <= 1.2676506002282294e30f) ||
            !(oB[10] >= 8.271806125530277e-25f && oB[10] <= 1.2676506002282294e30f);
    }
#pragma unroll
    for (int i = 0; i < 10; ++i) bad |= !(__builtin_fabsf(oA[i]) <= 3.0e38f) || !(__builtin_fabsf(oB[i]) <= 3.0e38f);
    if (__any(bad)) {
#pragma unroll 1
      for (int qq = 0; qq < 2; ++qq) {
        const bf16x8 qf = qq ? qB : qA;
        f32x16 oacc = zero16;
        float m = -INFINITY;
        for (int it = 0; it < 8; ++it) {
          const bf16x8 kx_ = ldsr128(xp + it * 512);
#pragma unroll
          for (int t = 0; t < 4; ++t) {
            const bf16x8 kf_ = ldsr128(kp + (it * 4 + t) * 1024);
            f32x16 s_ = MFMA32(kf_, qf, zero16);
            s_ = MFMA32(kx_, qq ? eB[t] : eA[t], s_);
            float tm = s_[0];
#pragma unroll
            for (int i = 1; i < 16; ++i) tm = fmaxf(tm, s_[i]);
            tm = fmaxf(tm, __shfl_xor(tm, 32));
            const float mn = fmaxf(m, tm);
            const float sc = EX2(m - mn);
            m = mn;
#pragma unroll
            for (int i = 0; i < 16; ++i) oacc[i] *= sc;
            const int ro_ = (it * 8 + t * 2) * (int)VREC;
            const bf16x8 v0_ = ldsr128(vp + ro_), v1_ = ldsr128(vp + ro_ + VREC);
            u32x4 pa_, pb_;
#pragma unroll
            for (int i = 0; i < 4; ++i) {
              pa_[i] = cvtpk(EX2(s_[2 * i] - m), EX2(s_[2 * i + 1] - m));
              pb_[i] = cvtpk(EX2(s_[8 + 2 * i] - m), EX2(s_[8 + 2 * i + 1] - m));
            }
            oacc = MFMA32(v0_, PF8(pa_), oacc);
            oacc = MFMA32(v1_, PF8(pb_), oacc);
          }
        }
        if (qq) { oB = oacc; mB = m; } else { oA = oacc; mA = m; }
      }
    }
  }
  const unsigned hm0 = eqmask(half, 0), hm1 = eqmask(half, 1);
  BARRIER();
  {
    f32x4* mb = (f32x4*)(lds + w * 4096);
    float os[11];
#pragma unroll
    for (int i = 0; i < 11; ++i) os[i] = sel4(oA[i], oB[i], 0.f, 0.f, hm1, hm0, 0u, 0u);
    const float ms = sel4(mA, mB, 0.f, 0.f, hm1, hm0, 0u, 0u);
    mb[lane] = (f32x4){os[0], os[1], os[2], os[3]};
    mb[64 + lane] = (f32x4){os[4], os[5], os[6], os[7]};
    mb[128 + lane] = (f32x4){os[8], os[9], os[10], ms};
  }
  BARRIER();
  {
    const f32x4* mb = (const f32x4*)(lds + (w ^ 4) * 4096);
    float ok[11];
#pragma unroll
    for (int i = 0; i < 11; ++i) ok[i] = sel4(oA[i], oB[i], 0.f, 0.f, hm0, hm1, 0u, 0u);
    const float mk = sel4(mA, mB, 0.f, 0.f, hm0, hm1, 0u, 0u);
    const f32x4 a0 = mb[lane], a1 = mb[64 + lane], a2 = mb[128 + lane];
    const float M = fmaxf(mk, a2[3]);
    const float s0 = EX2(mk - M), s1 = EX2(a2[3] - M);
    const float lsum = __shfl(ok[10] * s0 + a2[2] * s1, r);
    const float inv = 1.0f / lsum;
    const float c0 = s0 * inv, c1 = s1 * inv;
    u32x4 ov;
    ov[0] = cvtpk(ok[0] * c0 + a0[0] * c1, ok[1] * c0 + a0[1] * c1);
    ov[1] = cvtpk(ok[2] * c0 + a0[2] * c1, ok[3] * c0 + a0[3] * c1);
    ov[2] = cvtpk(ok[4] * c0 + a1[0] * c1, ok[5] * c0 + a1[1] * c1);
    ov[3] = cvtpk(ok[6] * c0 + a1[2] * c1, ok[7] * c0 + a1[3] * c1);
    const int T = qt * 256 + qp * 64 + half * 32 + r;
    char* ofb = ws + WS_OF + (size_t)(T >> 4) * 18432;
    st16(ofb + (h >> 1) * 1024 + (((h & 1) * 2 + hh) * 16 + (T & 15)) * 16, ov);
    if (hh == 0)
      st4(ofb + (16 + (h >> 4)) * 1024 + (((h & 15) >> 2) * 16 + (T & 15)) * 16 + (h & 3) * 4,
          cvtpk(ok[8] * c0 + a2[0] * c1, ok[9] * c0 + a2[1] * c1));
  }
  STAMP(1, 4);
  asm volatile("s_waitcnt vmcnt(0)" ::: "memory");
#undef TILE
#undef SIDE
#undef PFETCH
#undef STAGEB
#undef EX4
#undef CV2
}

__global__ __launch_bounds__(576, 1) void k3_proj(const u16* __restrict__ x, char* __restrict__ ws) {
  __shared__ __attribute__((aligned(16))) char lds[36864 + 9 * 256 + 256];
  const int tid = threadIdx.x, lane = tid & 63;
  const int w = __builtin_amdgcn_readfirstlane(tid >> 6);
  const int b = blockIdx.x, xg = b & 7, o = b >> 3;
  const int tt32 = xg * 8 + (o >> 2), ct = o & 3;
  const int g = lane >> 4, c16 = lane & 15;
  const int nt = ct * 9 + w;
  const unsigned lds0 = (unsigned)(uintptr_t)lds;
  STAMP(2, 0);
#pragma unroll
  for (int i = 0; i < 4; ++i)
    glds16(ws + WS_OF + (size_t)(tt32 * 2) * 18432 + (size_t)(i * 9 + w) * 1024, (unsigned)lane * 16u, lds0 + (unsigned)(i * 9 + w) * 1024u);
  const char* wfp = ws + WS_WOF + (size_t)nt * 18432 + lane * 16;
  bf16x8 bfr[18];
#pragma unroll
  for (int ks = 0; ks < 6; ++ks) bfr[ks] = *(const bf16x8*)(wfp + ks * 1024);
  asm volatile("s_waitcnt vmcnt(6)\n\ts_barrier" ::: "memory");
#pragma unroll
  for (int ks = 6; ks < 18; ++ks) bfr[ks] = *(const bf16x8*)(wfp + ks * 1024);
  u16 xr[2][4];
#pragma unroll
  for (int t = 0; t < 2; ++t)
#pragma unroll
    for (int j = 0; j < 4; ++j) xr[t][j] = x[(size_t)(tt32 * 32 + t * 16 + g * 4 + j) * 576 + nt * 16 + c16];
  SB0();
  f32x4 acc[2], acd[2];
  acc[0] = acc[1] = acd[0] = acd[1] = (f32x4){0, 0, 0, 0};
#pragma unroll
  for (int ks = 0; ks < 18; ks += 2) {
    acc[0] = MFMA16(ldsr128((lds_cptr)lds + ks * 1024 + lane * 16), bfr[ks], acc[0]);
    acc[1] = MFMA16(ldsr128((lds_cptr)lds + 18432 + ks * 1024 + lane * 16), bfr[ks], acc[1]);
    acd[0] = MFMA16(ldsr128((lds_cptr)lds + (ks + 1) * 1024 + lane * 16), bfr[ks + 1], acd[0]);
    acd[1] = MFMA16(ldsr128((lds_cptr)lds + 18432 + (ks + 1) * 1024 + lane * 16), bfr[ks + 1], acd[1]);
  }
  float* hp = (float*)(ws + WS_H);
  float* pst = (float*)(lds + 36864);
  float* fin = (float*)(lds + 36864 + 9 * 256);
  float sm[8], m2[8];
#pragma unroll
  for (int i = 0; i < 8; ++i) {
    const float hv = acc[i >> 2][i & 3] + acd[i >> 2][i & 3] + bf2f(xr[i >> 2][i & 3]);
    acc[i >> 2][i & 3] = hv; sm[i] = hv;
  }
#define DPPADD(v, c) v += __builtin_bit_cast(float, __builtin_amdgcn_update_dpp(0, __builtin_bit_cast(int, v), c, 0xf, 0xf, false))
#define ROW16(a) do { _Pragma("unroll") for (int i = 0; i < 8; ++i) DPPADD(a[i], 0x128); SB0(); _Pragma("unroll") for (int i = 0; i < 8; ++i) DPPADD(a[i], 0x124); SB0(); \
    _Pragma("unroll") for (int i = 0; i < 8; ++i) DPPADD(a[i], 0x122); SB0(); _Pragma("unroll") for (int i = 0; i < 8; ++i) DPPADD(a[i], 0x121); SB0(); } while (0)
  SB0();
  ROW16(sm);
#pragma unroll
  for (int i = 0; i < 8; ++i) { const float d = acc[i >> 2][i & 3] - sm[i] * (1.f / 16.f); m2[i] = d * d; }
  SB0();
  ROW16(m2);
  if (c16 == 0) {
#pragma unroll
    for (int t = 0; t < 2; ++t) {
      *(f32x4*)(pst + (w * 32 + t * 16 + g * 4) * 2) = (f32x4){sm[4 * t], m2[4 * t], sm[4 * t + 1], m2[4 * t + 1]};
      *(f32x4*)(pst + (w * 32 + t * 16 + g * 4) * 2 + 4) = (f32x4){sm[4 * t + 2], m2[4 * t + 2], sm[4 * t + 3], m2[4 * t + 3]};
    }
  }
#undef ROW16
#undef DPPADD
  STAMP(2, 1);
  BARRIER();
#define HSTORE() do { float* tb_ = (float*)(lds + w * 2048); \
    _Pragma("unroll") for (int i = 0; i < 8; ++i) tb_[(i >> 2) * 256 + ((c16 >> 2) * 16 + g * 4 + (i & 3)) * 4 + (c16 & 3)] = acc[i >> 2][i & 3]; \
    _Pragma("unroll") for (int t = 0; t < 2; ++t) \
      st16(hp + (size_t)((tt32 * 2 + t) * 36 + nt) * 256 + lane * 4, *(const u32x4*)(tb_ + t * 256 + lane * 4)); } while (0)
  if (w != 0) HSTORE();
  if (w == 0) {
    const int tok = lane & 31, st = lane >> 5;
    float sw[9], S = 0.f, M2 = 0.f;
#pragma unroll
    for (int i = 0; i < 9; ++i) { sw[i] = pst[(i * 32 + tok) * 2]; M2 += pst[(i * 32 + tok) * 2 + 1]; S += sw[i]; }
    const float mub = S * (1.f / 144.f);
#pragma unroll
    for (int i = 0; i < 9; ++i) { const float d = sw[i] * (1.f / 16.f) - mub; M2 += 16.f * d * d; }
    gu64* gr = (gu64*)(ws + WS_GRAN + (size_t)tt32 * 2048);
    __hip_atomic_store(gr + ct * 64 + lane, (1ull << 32) | (unsigned long long)__float_as_uint(st ? M2 : S), RLX_AGENT);
    float pv[4];
    for (unsigned spins = 0;; ++spins) {
      bool ok = true;
#pragma unroll
      for (int i = 0; i < 4; ++i) {
        const unsigned long long v = __hip_atomic_load(gr + i * 64 + lane, RLX_AGENT);
        pv[i] = __uint_as_float((unsigned)v);
        ok &= (v >> 32) == 1ull;
      }
      if (__all(ok) || spins > (1u << 22)) break;
      __builtin_amdgcn_s_sleep(1);
    }
    const float tot = (pv[0] + pv[1]) + (pv[2] + pv[3]);
    const float mu = tot * (1.f / 576.f);
    float extra = 0.f;
#pragma unroll
    for (int i = 0; i < 4; ++i) { const float d = pv[i] * (1.f / 144.f) - mu; extra += 144.f * d * d; }
    const float other = __shfl_xor(tot, 32);
    const float var = (other + extra) * (1.f / 576.f);
    const float r1 = rsqrtf(var + LN_EPS);
    const float r2 = rsqrtf(var * r1 * r1 + LN_EPS);
    if (st == 0) { fin[tok * 2] = mu; fin[tok * 2 + 1] = r1 * r2; }
  }
  BARRIER();
  STAMP(2, 2);
  char* hnp = ws + WS_HN;
#pragma unroll
  for (int t = 0; t < 2; ++t) {
    const f32x4 f0 = *(const f32x4*)(fin + (t * 16 + g * 4) * 2), f1 = *(const f32x4*)(fin + (t * 16 + g * 4) * 2 + 4);
    u32x2 v;
    v[0] = cvtpk((acc[t][0] - f0[0]) * f0[1], (acc[t][1] - f0[2]) * f0[3]);
    v[1] = cvtpk((acc[t][2] - f1[0]) * f1[1], (acc[t][3] - f1[2]) * f1[3]);
    st8(hnp + ((size_t)(nt * 64 + tt32) * 64 + (2 * t + (g >> 1)) * 16 + c16) * 16 + (g & 1) * 8, v);
  }
  if (w == 0) HSTORE();
  STAMP(2, 3);
#undef HSTORE
}

__device__ __forceinline__ float gelu_tanh(float v) {
  const float z = v * (0.7978845608028654f + 0.035677408136300125f * v * v);
  const float e = __builtin_amdgcn_exp2f(z * -2.8853900817779268f);
  return v * __builtin_amdgcn_rcpf(1.0f + e);
}
constexpr int G1_SB = 28672;
template <int MT>
__device__ __forceinline__ void g1_body(char* lds, const u16* __restrict__ w1, const u16* __restrict__ b1, char* __restrict__ ws,
                                        int lane, int w8, int dt0, int pn) {
  const int w = w8 & 3;
  const unsigned lds0 = (unsigned)(uintptr_t)lds;
  const int wr = w >> 1, wc = w & 1;
  const int g = lane >> 4, c16 = lane & 15;
  f32x4 acc[4][MT];
  if (w8 >= 4) {
    const char* hn = ws + WS_HN + (size_t)dt0 * 65536;
    unsigned aoff[MT];
#pragma unroll
    for (int i = 0; i < MT; ++i) { const int a = w + 4 * i; aoff[i] = (unsigned)((a >> 1) * 65536 + (a & 1) * 1024 + lane * 16); }
    const char* wb = (const char*)w1 + (size_t)((pn >> 2) * 8 + (pn & 3)) * 128;
    unsigned boff[4];
#pragma unroll
    for (int i = 0; i < 4; ++i) {
      const int row = (4 * w + i) * 4 + (lane >> 4), cp = lane & 15;
      const int f = (row & 3) | (((row >> 3) & 1) << 2);
      const int ch = (((cp >> 1) ^ f) << 1) | (cp & 1);
      boff[i] = (unsigned)(row * 9216 + (ch & 7) * 16 + (ch >> 3) * 512);
    }
#define G1_ISSUE(t) do { const unsigned sl_ = lds0 + (unsigned)((t) & 3) * G1_SB; \
    const char* ab_ = hn + (size_t)(t) * 2048; const char* bb_ = wb + (size_t)(t) * (64 * 9216); \
    _Pragma("unroll") for (int i = 0; i < 4; ++i) glds16(bb_, boff[i], sl_ + 12288u + (unsigned)(4 * w + i) * 1024u); \
    _Pragma("unroll") for (int i = 0; i < MT; ++i) glds16(ab_, aoff[i], sl_ + (unsigned)(w + 4 * i) * 1024u); } while (0)
#define G1_WAIT(n) asm volatile("s_waitcnt vmcnt(%0)\n\ts_barrier" :: "i"(n) : "memory")
    G1_ISSUE(0); G1_ISSUE(1); G1_ISSUE(2);
#pragma unroll 1
    for (int t = 0; t < 29; ++t) { G1_WAIT(2 * (4 + MT)); G1_ISSUE(t + 3); }
    G1_WAIT(2 * (4 + MT)); G1_WAIT(4 + MT); G1_WAIT(0);
    BARRIER();
    BARRIER();
#pragma unroll
    for (int n = 0; n < 2; ++n)
#pragma unroll
      for (int m = 0; m < MT; ++m) acc[n][m] = *(const f32x4*)(lds + ((w * 2 + n) * MT + m) * 1024 + lane * 16);
  } else {
    CODE_TOUCH(SYM_K4, SYM_K5, w + 4 * (dt0 & 1), lds0 + 4 * G1_SB);
    const int q4 = (lane & 15) >> 2, p4 = lane & 3;
    const int fl = q4 | ((g & 1) << 2);
    lds_cptr bp0[4], bp1[4];
#pragma unroll
    for (int n = 0; n < 4; ++n) {
      bp0[n] = (lds_cptr)lds + (12288 + (8 * g + q4) * 256 + (((wc * 4 + n) ^ fl) * 32) + 8 * p4);
      bp1[n] = bp0[n] + 2 * G1_SB;
      asm volatile("" : "+v"(bp1[n]));
    }
    const lds_cptr ap0 = (lds_cptr)lds + (wr * MT * 2048 + lane * 16);
    lds_cptr ap1 = ap0 + 2 * G1_SB;
    asm volatile("" : "+v"(ap1));
#pragma unroll
    for (int n = 0; n < 4; ++n)
#pragma unroll
      for (int m = 0; m < MT; ++m) acc[n][m] = (f32x4){0, 0, 0, 0};
    bf16x8 aX[MT], bX[4], aY[MT], bY[4];
#define G1_READ(A_, B_, sl, ks) do { const lds_cptr a_ = ((sl) < 2 ? ap0 : ap1) + ((sl) & 1) * G1_SB + (ks) * 1024; \
    _Pragma("unroll") for (int m = 0; m < MT; ++m) A_[m] = ldsr128(a_ + m * 2048); \
    _Pragma("unroll") for (int n = 0; n < 4; ++n) { const lds_cptr b_ = ((sl) < 2 ? bp0[n] : bp1[n]) + ((sl) & 1) * G1_SB + (ks) * 8192; \
      B_[n] = cat8(trread(b_), trread(b_ + 1024)); } } while (0)
#define G1_MMA(A_, B_) do { _Pragma("unroll") for (int n = 0; n < 4; ++n) _Pragma("unroll") for (int m = 0; m < MT; ++m) \
    acc[n][m] = MFMA16(B_[n], A_[m], acc[n][m]); } while (0)
#define G1_ILV() do { _Pragma("unroll") for (int i_ = 0; i_ < 6; ++i_) { SGB(0x8, 1); SGB(0x100, 2); } SGB(0x8, 4 * MT - 6); } while (0)
#define G1_STAGE(sl) do { \
    G1_READ(aY, bY, sl, 1); G1_MMA(aX, bX); G1_ILV(); SB0(); \
    BARRIER(); SB0(); \
    G1_READ(aX, bX, ((sl) + 1) & 3, 0); G1_MMA(aY, bY); G1_ILV(); SB0(); } while (0)
    BARRIER();
    STAMP(3, 1);
    G1_READ(aX, bX, 0, 0);
    SB0();
#pragma unroll 1
    for (int t = 0; t < 8; ++t) { G1_STAGE(0); G1_STAGE(1); G1_STAGE(2); G1_STAGE(3); }
    STAMP(3, 3);
    __builtin_amdgcn_s_waitcnt(0xC07F);
#pragma unroll
    for (int n = 0; n < 2; ++n)
#pragma unroll
      for (int m = 0; m < MT; ++m) *(f32x4*)(lds + ((w * 2 + n) * MT + m) * 1024 + lane * 16) = acc[2 + n][m];
    BARRIER();
  }
  char* up = ws + WS_U;
#pragma unroll
  for (int n = 0; n < 2; ++n) {
    const int ft = ((pn >> 2) * 8 + (pn & 3) + 4 * wc) * 4 + (w8 >> 2) * 2 + n;
    const u32x2 bv = *(const u32x2*)(b1 + ft * 16 + g * 4);
    const float bb0 = __uint_as_float(bv[0] << 16), bb1 = __uint_as_float(bv[0] & 0xffff0000u);
    const float bb2 = __uint_as_float(bv[1] << 16), bb3 = __uint_as_float(bv[1] & 0xffff0000u);
#pragma unroll
    for (int m = 0; m < MT; ++m) {
      const int dt = dt0 + wr * MT + m;
      u32x2 v;
      v[0] = cvtpk(gelu_tanh(acc[n][m][0] + bb0), gelu_tanh(acc[n][m][1] + bb1));
      v[1] = cvtpk(gelu_tanh(acc[n][m][2] + bb2), gelu_tanh(acc[n][m][3] + bb3));
      st8(up + ((size_t)(dt * 144 + (ft >> 1)) * 64 + ((ft & 1) * 2 + (g >> 1)) * 16 + c16) * 16 + (g & 1) * 8, v);
    }
  }
  STAMP(3, 4);
  asm volatile("s_waitcnt vmcnt(0)" ::: "memory");
#undef G1_ISSUE
#undef G1_WAIT
#undef G1_READ
#undef G1_MMA
#undef G1_ILV
#undef G1_STAGE
}
__global__ __launch_bounds__(512, 1) void k4_gemm1(const u16* __restrict__ w1, const u16* __restrict__ b1, const u16* __restrict__ w2,
                                                    char* __restrict__ ws) {
  __shared__ __attribute__((aligned(16))) char lds[4 * G1_SB + 256];
  const int tid = threadIdx.x, lane = tid & 63;
  const int w8 = __builtin_amdgcn_readfirstlane(tid >> 6);
  const int id = (blockIdx.x & 7) * 32 + (blockIdx.x >> 3);
  const int sid = id - (id >> 5) * 2 + ((id >> 5) * 3) / 8;
  const bool spare = (id & 31) >= 30 + ((((id >> 5) + 1) * 3) / 8 - ((id >> 5) * 3) / 8);
  if (spare) {
    const int xg = id >> 5, sp = 2 * xg - (xg * 3) / 8 + ((id & 31) - (30 + (((xg + 1) * 3) / 8 - (xg * 3) / 8)));
    const unsigned lds0 = (unsigned)(uintptr_t)lds;
    const char* p = (const char*)w2 + (size_t)(NW2 * 2048 + sp * 8 + w8) * 1024;
#pragma unroll 1
    for (int j = NW2 * 2048 + sp * 8 + w8; j < 18432; j += 104) {
      glds16(p, (unsigned)lane * 16u, lds0 + (unsigned)w8 * 1024u);
      p += 104 * 1024;
      asm volatile("s_waitcnt vmcnt(" STR_(HTHR) ")" ::: "memory");
    }
    asm volatile("s_waitcnt vmcnt(0)" ::: "memory");
    return;
  }
  const int grp = sid / 27, r = sid - grp * 27;
  STAMP(3, 0);
  asm volatile("" :: "s"(b1), "s"(ws));
  if (r < 18) g1_body<3>(lds, w1, b1, ws, lane, w8, (r % 6) * 6, grp * 4 + r / 6);
  else        g1_body<2>(lds, w1, b1, ws, lane, w8, (r - 18) * 4, grp * 4 + 3);
}

constexpr int G2_ST = 4, G2_SB = 36864;
__global__ __launch_bounds__(512, 1) void k5_gemm2(const u16* __restrict__ w2, const u16* __restrict__ b2, char* __restrict__ ws,
                                                    u16* __restrict__ out) {
  __shared__ __attribute__((aligned(16))) char lds[G2_ST * G2_SB + 256];
  const int tid = threadIdx.x, lane = tid & 63;
  const int w8 = __builtin_amdgcn_readfirstlane(tid >> 6), w = w8 & 3;
  const int xg = blockIdx.x & 7, o = blockIdx.x >> 3;
  const int kq = xg & 3, pm = o & 3, pn = (xg >> 2) * 8 + (o >> 2);
  const int tile = pn * 4 + pm;
  const unsigned lds0 = (unsigned)(uintptr_t)lds;
  STAMP(4, 0);
  if (w8 >= 4) {
    const char* ub = ws + WS_U + (size_t)(pm * 9) * 147456 + (size_t)kq * 36864;
    unsigned aoff[5];
#pragma unroll
    for (int i = 0; i < 5; ++i) { int a = w + 4 * i; if (a >= 18) a -= 4; aoff[i] = (unsigned)((a >> 1) * 147456 + (a & 1) * 1024 + lane * 16); }
    const char* wb = (const char*)w2 + (size_t)kq * 1152 * 4096 + (size_t)((pn >> 2) * 8 + (pn & 3)) * 128;
    unsigned boff[4];
#pragma unroll
    for (int i = 0; i < 4; ++i) {
      const int row = (4 * w + i) * 4 + (lane >> 4), cp = lane & 15;
      const int f = (row & 3) | (((row >> 3) & 1) << 2);
      const int ch = (((cp >> 1) ^ f) << 1) | (cp & 1);
      boff[i] = (unsigned)(row * 4096 + (ch & 7) * 16 + (ch >> 3) * 512);
    }
#define G2_ISSUE(NA, t, slot) do { const unsigned sl_ = lds0 + (unsigned)(slot) * G2_SB; \
    const char* ab_ = ub + (size_t)(t) * 2048; const char* bb_ = wb + (size_t)(t) * (64 * 4096); \
    _Pragma("unroll") for (int i = 0; i < 4; ++i) glds16(bb_, boff[i], sl_ + 20480u + (unsigned)(4 * w + i) * 1024u); \
    _Pragma("unroll") for (int i = 0; i < NA; ++i) glds16(ab_, aoff[i], sl_ + (unsigned)(w + 4 * i) * 1024u); } while (0)
#define G2_LOADER(NA, W2_, W1_) do { \
    _Pragma("unroll") for (int t = 0; t < G2_ST - 1; ++t) G2_ISSUE(NA, t, t); \
    int islot = G2_ST - 1; \
    for (int t = 0; t < 18 - (G2_ST - 1); ++t) { \
      asm volatile("s_waitcnt vmcnt(" #W2_ ")\n\ts_barrier" ::: "memory"); \
      G2_ISSUE(NA, t + G2_ST - 1, islot); \
      islot = (islot + 1 == G2_ST) ? 0 : islot + 1; } \
    asm volatile("s_waitcnt vmcnt(" #W2_ ")\n\ts_barrier" ::: "memory"); \
    _Pragma("unroll") for (int i4 = 0; i4 < 5; ++i4) {         \
      const int tau_ = ((i4 == 4 && kq >= 2) ? 3 : i4) * 4 + kq; \
      const int st_ = ((pn >> 2) * 8 + (pn & 3) + 4 * (w >> 1)) * 4 + (w & 1) * 2 + (tau_ & 1); \
      glds16(ws + WS_H + (size_t)(st_ * 36 + pm * 9 + (tau_ >> 1)) * 1024, (unsigned)lane * 16u, lds0 + 2u * G2_SB + (unsigned)(w * 5 + i4) * 1024u); } \
    asm volatile("s_waitcnt vmcnt(" #W1_ ")\n\ts_barrier" ::: "memory"); \
    asm volatile("s_waitcnt vmcnt(0)\n\ts_barrier" ::: "memory"); } while (0)
    if (w < 2) G2_LOADER(5, 18, 14); else G2_LOADER(4, 16, 13);
    return;
  }
  CODE_TOUCH(SYM_K5, SYM_END, w, lds0 + G2_ST * G2_SB);
  const int g = lane >> 4, c16 = lane & 15, q4 = (lane & 15) >> 2, p4 = lane & 3;
  const int fl = q4 | ((g & 1) << 2);
  unsigned brd[2];
#pragma unroll
  for (int n = 0; n < 2; ++n) brd[n] = (unsigned)(20480 + (8 * g + q4) * 256 + (((w * 2 + n) ^ fl) * 32) + 8 * p4);
  const unsigned ard = (unsigned)(lane * 16);
  u16 bb[5]; unsigned oo[5];
#pragma unroll
  for (int i4 = 0; i4 < 5; ++i4) {
    const int ie = (i4 == 4 && kq >= 2) ? 3 : i4;
    const int tau = ie * 4 + kq, m = tau >> 1, n = tau & 1;
    const int sp_ = ((pn >> 2) * 8 + (pn & 3) + 4 * (w >> 1)) * 64 + ((w & 1) * 2 + n) * 16 + c16, d0 = pm * 144 + m * 16 + g * 4;
    oo[i4] = (unsigned)(sp_ * 576 + d0);
    bb[i4] = b2[sp_];
  }
  f32x4 acc[18];
#pragma unroll
  for (int i = 0; i < 18; ++i) acc[i] = (f32x4){0, 0, 0, 0};
  int slot = 0;
  bf16x8 aX[9], bX[2], aY[9], bY[2];
#define G2_READ(A_, B_, ks) do { const lds_cptr sp_ = (lds_cptr)lds + slot * G2_SB; \
    _Pragma("unroll") for (int n = 0; n < 2; ++n) B_[n] = cat8(trread(sp_ + brd[n] + (ks) * 8192), trread(sp_ + brd[n] + (ks) * 8192 + 1024)); \
    _Pragma("unroll") for (int m = 0; m < 9; ++m) A_[m] = ldsr128(sp_ + ard + m * 2048 + (ks) * 1024); } while (0)
#define G2_MMA(A_, B_) do { _Pragma("unroll") for (int m = 0; m < 9; ++m) { \
    acc[m * 2] = MFMA16(A_[m], B_[0], acc[m * 2]); acc[m * 2 + 1] = MFMA16(A_[m], B_[1], acc[m * 2 + 1]); } } while (0)
#define G2_ILV() do { _Pragma("unroll") for (int i_ = 0; i_ < 13; ++i_) { SGB(0x8, 1); SGB(0x100, 1); } SGB(0x8, 5); } while (0)
  asm volatile("" :: "s"(b2), "s"(ws), "s"(out));
  BARRIER();
  STAMP(4, 1);
  __builtin_amdgcn_s_waitcnt(0xC07F);
  G2_READ(aX, bX, 0);
  SB0();
  for (int t = 0; t < 17; ++t) {
    G2_READ(aY, bY, 1); G2_MMA(aX, bX); G2_ILV(); SB0();
    BARRIER();
    slot = (slot + 1 == G2_ST) ? 0 : slot + 1;
    SB0();
    G2_READ(aX, bX, 0); G2_MMA(aY, bY); G2_ILV(); SB0();
  }
  G2_READ(aY, bY, 1); G2_MMA(aX, bX); G2_ILV(); SB0();
  G2_MMA(aY, bY);
  STAMP(4, 3);
  const auto rsrc = __builtin_amdgcn_make_buffer_rsrc(ws + WS_SLAB + (size_t)tile * SLABT, 0, SLABT, 0x00020000);
#define PKW(i, e) __builtin_elementwise_min(cvtpk(acc[i][2 * (e)], acc[i][2 * (e) + 1]), 0xFFFFFFFEu)
#pragma unroll
  for (int dst = 0; dst < 4; ++dst) {
    if (dst != kq) {
      const int sb = (dst * 4 + kq) * 10240 + w * 2560;
      __builtin_amdgcn_raw_buffer_store_b128((u32x4){PKW(dst, 0), PKW(dst, 1), PKW(dst + 4, 0), PKW(dst + 4, 1)}, rsrc, sb + lane * 16, 0, 16);
      __builtin_amdgcn_raw_buffer_store_b128((u32x4){PKW(dst + 8, 0), PKW(dst + 8, 1), PKW(dst + 12, 0), PKW(dst + 12, 1)}, rsrc, sb + 1024 + lane * 16, 0, 16);
      if (dst < 2) __builtin_amdgcn_raw_buffer_store_b64((u32x2){PKW((dst + 16) % 18, 0), PKW((dst + 16) % 18, 1)}, rsrc, sb + 2048 + lane * 8, 0, 16);
    }
  }
#undef PKW
  STAMP(4, 4);
  f32x4 own[5];
  const unsigned km0 = eqmask(kq, 0), km1 = eqmask(kq, 1), km2 = eqmask(kq, 2), km3 = eqmask(kq, 3);
#pragma unroll
  for (int i4 = 0; i4 < 5; ++i4) {
#pragma unroll
    for (int j = 0; j < 4; ++j)
      own[i4][j] = sel4(acc[i4 * 4][j], acc[i4 * 4 + 1][j], acc[(i4 * 4 + 2) % 18][j], acc[(i4 * 4 + 3) % 18][j], km0, km1, km2, km3);
    own[i4] += *(const f32x4*)(lds + 2 * G2_SB + (w * 5 + i4) * 1024 + lane * 16) + bf2f(bb[i4]);
  }
  asm volatile("s_waitcnt vmcnt(0)" ::: "memory");
  u32x4 pa[3], pb[3]; u32x2 pc[3];
  const int x8 = (kq < 2) ? 2048 + lane * 8 : lane * 16;
  for (unsigned spins = 0;; ++spins) {
#pragma unroll
    for (int s = 0; s < 3; ++s) {
      const int sb = (kq * 4 + ((kq + s + 1) & 3)) * 10240 + w * 2560;
      pa[s] = __builtin_amdgcn_raw_buffer_load_b128(rsrc, sb + lane * 16, 0, 16);
      pb[s] = __builtin_amdgcn_raw_buffer_load_b128(rsrc, sb + 1024 + lane * 16, 0, 16);
      pc[s] = __builtin_amdgcn_raw_buffer_load_b64(rsrc, sb + x8, 0, 16);
    }
    unsigned mx = 0;
#pragma unroll
    for (int s = 0; s < 3; ++s) {
#pragma unroll
      for (int e = 0; e < 4; ++e) mx = max(mx, max(pa[s][e], pb[s][e]));
      mx = max(mx, max(pc[s][0], pc[s][1]));
    }
    if (__all(mx != 0xFFFFFFFFu) || spins > (1u << 20)) break;
    __builtin_amdgcn_s_sleep(2);
  }
  STAMP(4, 5);
#define UPK(d) (f32x4){__uint_as_float((d)[0] << 16), __uint_as_float((d)[0] & 0xffff0000u), __uint_as_float((d)[1] << 16), __uint_as_float((d)[1] & 0xffff0000u)}
#pragma unroll
  for (int i4 = 0; i4 < 5; ++i4) {
    f32x4 a = own[i4];
#pragma unroll
    for (int s = 0; s < 3; ++s) {
      const u32x2 d = (i4 == 0) ? (u32x2){pa[s][0], pa[s][1]} : (i4 == 1) ? (u32x2){pa[s][2], pa[s][3]} :
                      (i4 == 2) ? (u32x2){pb[s][0], pb[s][1]} : (i4 == 3) ? (u32x2){pb[s][2], pb[s][3]} : pc[s];
      a += UPK(d);
    }
    if (i4 < 4 || kq < 2) *(u32x2*)(out + oo[i4]) = (u32x2){cvtpk(a[0], a[1]), cvtpk(a[2], a[3])};
  }
#undef UPK
  STAMP(4, 6);
#undef G2_ISSUE
#undef G2_READ
#undef G2_MMA
#undef G2_LOADER
}

__global__ void k_end() {}

extern "C" void kernel_launch(void* const* d_in, const int* in_sizes, int n_in,
                              void* d_out, int out_size, void* d_ws, size_t ws_size,
                              hipStream_t stream) {
  const u16* x    = (const u16*)d_in[0];
  const u16* wqkv = (const u16*)d_in[1];
  const u16* wo   = (const u16*)d_in[2];
  const u16* w1   = (const u16*)d_in[4];
  const u16* b1   = (const u16*)d_in[5];
  const u16* w2   = (const u16*)d_in[6];
  const u16* b2   = (const u16*)d_in[7];
  char* ws = (char*)d_ws;
  k1_qkv<<<256, 512, 0, stream>>>(x, wqkv, wo, ws);
  k2_attn<<<256, 512, 0, stream>>>(ws, w1, w2);
  k3_proj<<<256, 576, 0, stream>>>(x, ws);
  k4_gemm1<<<256, 512, 0, stream>>>(w1, b1, w2, ws);
  k5_gemm2<<<256, 512, 0, stream>>>(w2, b2, ws, (u16*)d_out);
}
```

```cpp
#include <hip/hip_runtime.h>
#include <stdint.h>

typedef short    bf16x8 __attribute__((ext_vector_type(8)));
typedef short    s16x4  __attribute__((ext_vector_type(4)));
typedef float    f32x4  __attribute__((ext_vector_type(4)));
typedef float    f32x16 __attribute__((ext_vector_type(16)));
typedef unsigned u32x2  __attribute__((ext_vector_type(2)));
typedef unsigned u32x4  __attribute__((ext_vector_type(4)));
typedef __bf16   bf16x2_t __attribute__((ext_vector_type(2)));
typedef unsigned short u16;
#define LDS_AS __attribute__((address_space(3)))
#define GLB_AS __attribute__((address_space(1)))
typedef LDS_AS const char* lds_cptr;
typedef GLB_AS unsigned gu32;
typedef GLB_AS unsigned long long gu64;
#define RLX_AGENT __ATOMIC_RELAXED, __HIP_MEMORY_SCOPE_AGENT

constexpr int S_ = 2048, D_ = 576, F_ = 4608;
constexpr float LN_EPS = 1e-5f;
constexpr float QSCALE = 0.23570226039551584f * 1.4426950408889634f;

constexpr unsigned VREC    = 640;
constexpr unsigned KXOFF   = 65536 + 128 * VREC;
constexpr unsigned KVHEAD  = KXOFF + 8192;
constexpr size_t WS_QM   = 0;
constexpr size_t WS_QX   = WS_QM + 32u * 65536;
constexpr size_t WS_KV   = WS_QX + 32u * 8192;
constexpr size_t WS_OF   = WS_KV + 32u * KVHEAD;
constexpr size_t WS_WOF  = WS_OF + 128u * 18 * 1024;
constexpr size_t WS_HN   = WS_WOF + 36u * 18 * 1024;
constexpr size_t WS_H    = WS_HN + 36u * 64 * 1024;
constexpr size_t WS_U    = WS_H + 2048u * 576 * 4;
constexpr size_t WS_GRAN = WS_U + 36u * 144 * 1024;
constexpr size_t WS_SLAB = WS_GRAN + 64u * 4 * 512;
constexpr unsigned SLABT = 16u * 10240;

__device__ __forceinline__ unsigned cvtpk(float lo, float hi) {
  typedef float f32x2_t __attribute__((ext_vector_type(2)));
  return __builtin_bit_cast(unsigned, __builtin_convertvector((f32x2_t){lo, hi}, bf16x2_t));
}
__device__ __forceinline__ float bf2f(u16 v) { return __uint_as_float((unsigned)v << 16); }
__device__ __forceinline__ void glds16(const void* sbase, unsigned voff, unsigned lds_base) {
  unsigned sv;
  asm volatile("s_mov_b32 %0, m0\n\ts_mov_b32 m0, %3\n\ts_nop 0\n\tglobal_load_lds_dwordx4 %1, %2\n\ts_mov_b32 m0, %0"
               : "=&s"(sv) : "v"(voff), "s"(sbase), "s"(lds_base) : "memory");
}
__device__ __forceinline__ void touch4(const void* sbase, unsigned voff, unsigned lds_base) {
  unsigned sv;
  asm volatile("s_mov_b32 %0, m0\n\ts_mov_b32 m0, %3\n\ts_nop 0\n\tglobal_load_lds_dword %1, %2\n\ts_mov_b32 m0, %0"
               : "=&s"(sv) : "v"(voff), "s"(sbase), "s"(lds_base) : "memory");
}
__device__ __forceinline__ s16x4 trread(lds_cptr p) {
  typedef short v4i16_t __attribute__((ext_vector_type(4)));
  return __builtin_bit_cast(s16x4, __builtin_amdgcn_ds_read_tr16_b64_v4i16((LDS_AS v4i16_t*)p));
}
__device__ __forceinline__ bf16x8 cat8(s16x4 a, s16x4 b) {
  return (bf16x8){a[0], a[1], a[2], a[3], b[0], b[1], b[2], b[3]};
}
__device__ __forceinline__ bf16x8 ldsr128(lds_cptr p) { return *(const LDS_AS bf16x8*)p; }
#define MFMA16(a, b, c) __builtin_amdgcn_mfma_f32_16x16x32_bf16((a), (b), (c), 0, 0, 0)
#define MFMA32(a, b, c) __builtin_amdgcn_mfma_f32_32x32x16_bf16((a), (b), (c), 0, 0, 0)
#define SB0() __builtin_amdgcn_sched_barrier(0)
#define SGB(mask, n) __builtin_amdgcn_sched_group_barrier((mask), (n), 0)
#define WAITV(n) asm volatile("s_waitcnt vmcnt(" #n ")" ::: "memory")
#define BARRIER() do { asm volatile("" ::: "memory"); __builtin_amdgcn_s_waitcnt(0xC07F); __builtin_amdgcn_s_barrier(); asm volatile("" ::: "memory"); } while (0)
__device__ __forceinline__ float dot2(unsigned a, unsigned b, float c) {
  return __builtin_amdgcn_fdot2_f32_bf16(__builtin_bit_cast(bf16x2_t, a), __builtin_bit_cast(bf16x2_t, b), c, false);
}
constexpr unsigned ONES2 = 0x3F803F80u;
__device__ __forceinline__ void st16(void* p, u32x4 v) { asm volatile("global_store_dwordx4 %0, %1, off sc1\n\ts_nop 1" :: "v"(p), "v"(v) : "memory"); }
__device__ __forceinline__ void st8(void* p, u32x2 v) { asm volatile("global_store_dwordx2 %0, %1, off sc1" :: "v"(p), "v"(v) : "memory"); }
__device__ __forceinline__ void st4(void* p, unsigned v) { asm volatile("global_store_dword %0, %1, off sc1" :: "v"(p), "v"(v) : "memory"); }
__device__ __forceinline__ float sel4(float a, float b, float c, float d, unsigned m0, unsigned m1, unsigned m2, unsigned m3) {
  return __uint_as_float((__float_as_uint(a) & m0) | (__float_as_uint(b) & m1) | (__float_as_uint(c) & m2) | (__float_as_uint(d) & m3));
}
__device__ __forceinline__ unsigned eqmask(int a, int b) { unsigned m = (a == b) ? ~0u : 0u; asm volatile("" : "+v"(m)); return m; }
#define STAMP(kid, i) do {} while (0)
#define TSTAMP(i) do {} while (0)
#define TFLUSH(kid, a, b) do {} while (0)

#define CODE_ADDR(dst, sym) do { unsigned lo_, hi_; \
    asm volatile("s_getpc_b64 s[98:99]\n\ts_add_u32 %0, s98, " sym "@rel32@lo+4\n\ts_addc_u32 %1, s99, " sym "@rel32@hi+12" \
                 : "=s"(lo_), "=s"(hi_) :: "s98", "s99", "scc"); \
    dst = (const char*)(((unsigned long long)hi_ << 32) | lo_); } while (0)

#define CODE_TOUCH(symA, symB, slot, ldsd) do { const char* ca_; const char* cb_; CODE_ADDR(ca_, symA); CODE_ADDR(cb_, symB); \
    const unsigned ns_ = (unsigned)(cb_ - ca_) >> 6; \
    for (unsigned c0_ = (unsigned)(slot) * 64u; c0_ < ns_; c0_ += 512u) { const unsigned ci_ = c0_ + lane; touch4(ca_, (ci_ < ns_ ? ci_ : 0u) * 64u, (ldsd)); } } while (0)
#define SYM_K1 "_Z6k1_qkvPKtS0_S0_S0_Pc"
#define PFK1 2
#define PFTAIL 1
#define SYM_K2 "_Z7k2_attnPcPKtS1_S1_"
#define SYM_K4 "_Z8k4_gemm1PKtS0_Pc"
#define SYM_K5 "_Z8k5_gemm2PKtS0_PcPt"
#define SYM_END "_Z5k_endv"

constexpr int Q_ST = 5, Q_SB = 30720;
__global__ __launch_bounds__(512, 1) void k1_qkv(const u16* __restrict__ x, const u16* __restrict__ wqkv, const u16* __restrict__ wo,
                                                  const u16* __restrict__ w1, char* __restrict__ ws) {
  __shared__ __attribute__((aligned(16))) char lds[Q_ST * Q_SB + 512 + 1024];
  const int tid = threadIdx.x, lane = tid & 63;
  const int w8 = __builtin_amdgcn_readfirstlane(tid >> 6), w = w8 & 3;
  const int b = blockIdx.x, xg = b & 7, o = b >> 3;
  const int bm = (xg >> 1) * 4 + (o & 3), hg = (xg & 1) * 8 + (o >> 2);
  const unsigned lds0 = (unsigned)(uintptr_t)lds;
  float* csl = (float*)(lds + Q_ST * Q_SB);
  constexpr unsigned DUMMY = Q_ST * Q_SB + 512;
  constexpr int SP = 304;
  u16 wog[8];
  STAMP(0, 0);
  if (w8 >= 4) {
    const char* xb = (const char*)x + (size_t)bm * 128 * 1152;
    const char* wb = (const char*)wqkv + (216 * hg - 8 * (hg & 1));
    unsigned xoff[4], woff[4];
#pragma unroll
    for (int i = 0; i < 4; ++i) {
      const int row = (w + 4 * i) * 8 + (lane >> 3);
      xoff[i] = (unsigned)(row * 1152 + (((lane & 7) ^ (lane >> 3)) * 16));
      int pc = w + 4 * i; if (pc >= 14) pc -= 4;
      const unsigned c = (unsigned)(pc * 64 + lane), pos = c / 14u, ch = c - pos * 14u;
      const unsigned k = (pos & ~12u) | ((pos & 4u) << 1) | ((pos & 8u) >> 1);
      woff[i] = k * 3456u + ch * 16u;
    }
    int it_ = 0; unsigned isl = lds0;
#define Q_ISSUE() do { const char* xb_ = xb + (size_t)it_ * 128; const char* wb_ = wb + (size_t)it_ * (64 * 3456); \
    _Pragma("unroll") for (int i = 0; i < 3; ++i) glds16(wb_, woff[i], isl + 16384u + (unsigned)(w + 4 * i) * 1024u); \
    if (w < 2) glds16(wb_, woff[3], isl + 16384u + (unsigned)(w + 12) * 1024u); \
    _Pragma("unroll") for (int i = 0; i < 4; ++i) glds16(xb_, xoff[i], isl + (unsigned)(w + 4 * i) * 1024u); \
    ++it_; isl = (isl + Q_SB == lds0 + Q_ST * Q_SB) ? lds0 : isl + Q_SB; } while (0)
#define Q_WAIT(NA, NB) do { if (w < 2) asm volatile("s_waitcnt vmcnt(" #NA ")\n\ts_barrier" ::: "memory"); \
                            else       asm volatile("s_waitcnt vmcnt(" #NB ")\n\ts_barrier" ::: "memory"); } while (0)
    Q_ISSUE(); Q_ISSUE(); Q_ISSUE();
    Q_WAIT(16, 14); Q_ISSUE(); Q_ISSUE();
#pragma unroll 1
    for (int t = 1; t < 5; ++t) { Q_WAIT(24, 21); Q_ISSUE(); }
    {
      const int tk = b * 162 + ((tid - 256 < 162) ? tid - 256 : 0);
      const int l_ = tk & 63, ks = (tk >> 6) % 18, nt = tk / (64 * 18);
      const int g_ = l_ >> 4, n_ = nt * 16 + (l_ & 15);
      const int k0 = (ks < 16) ? 18 * (2 * ks + (g_ >> 1)) + 4 * (g_ & 1) : 18 * (16 * (ks - 16) + 4 * g_) + 16;
      const u16* wp_ = wo + k0 * 576 + n_;
#pragma unroll
      for (int j = 0; j < 8; ++j) {
        const int dk = (ks < 16) ? (j & 3) + 8 * (j >> 2) : 18 * (j >> 1) + (j & 1);
        wog[j] = wp_[dk * 576];
      }
    }
    Q_WAIT(32, 29);
    Q_WAIT(24, 22);
    Q_WAIT(16, 15);
    Q_WAIT(8, 8);
#pragma unroll
    for (int i = 0; i < 3; ++i)
      if (i < 2 || tid - 256 < 64) st16(ws + WS_OF + (size_t)b * 9216 + (size_t)(i * 256 + tid - 256) * 16, (u32x4){~0u, ~0u, ~0u, ~0u});
    if (tid - 256 < 32) st16(ws + WS_GRAN + (size_t)b * 512 + (tid - 256) * 16, (u32x4){0, 0, 0, 0});
#pragma unroll
    for (int i = 0; i < 2 * PFK1; ++i)
      glds16((const char*)w1 + (size_t)(i * 1024 + b * 4 + w) * 1024, (unsigned)lane * 16u, lds0 + DUMMY);
    BARRIER();
    BARRIER();
  } else {
    const unsigned vw = (unsigned)(o * 4 + w);
    CODE_TOUCH(SYM_K1, SYM_K4, vw & 7u, lds0 + DUMMY);
    const int g = lane >> 4, c16 = lane & 15, q4 = (lane & 15) >> 2, p4 = lane & 3;
#define Q_TILE(i) ((w == 3) ? ((i) == 0 ? 6 : (i) - 1) : ((i) < 2 ? 2 * w + (i) : ((i) < 2 + 2 * w ? (i) - 2 : (i))))
    unsigned wrd[7], so[7][2]; float qs[7][2];
#pragma unroll
    for (int i = 0; i < 7; ++i) {
      const int n = Q_TILE(i);
      int Q = 4 * n + p4; if (Q > 26) Q = 26;
      wrd[i] = (unsigned)(16384 + ((g >> 1) * 16 + (g & 1) * 4 + q4) * 224 + 8 * Q + 8 * (hg & 1));
#pragma unroll
      for (int e = 0; e < 2; ++e) {
        const unsigned col = (unsigned)(n * 16 + g * 4 + e * 2);
        const unsigned hl = col / 18u;
        so[i][e] = (unsigned)((w * 32 + c16) * SP) + col * 2u + hl * 12u;
        qs[i][e] = ((unsigned)hg * 6u + hl < 32u) ? QSCALE : 1.0f;
        asm volatile("" : "+v"(so[i][e]), "+v"(qs[i][e]));
      }
    }
    const unsigned xrd0 = (unsigned)((w * 32 + c16) * 128 + ((g ^ (c16 & 7)) * 16)), xrd1 = xrd0 ^ 64u;
    f32x4 acc[7][2], csa[2], sq[2], sx[2];
#pragma unroll
    for (int n = 0; n < 7; ++n) { acc[n][0] = (f32x4){0, 0, 0, 0}; acc[n][1] = (f32x4){0, 0, 0, 0}; }
#pragma unroll
    for (int i = 0; i < 2; ++i) { csa[i] = (f32x4){0, 0, 0, 0}; sq[i] = (f32x4){0, 0, 0, 0}; sx[i] = (f32x4){0, 0, 0, 0}; }
    const bf16x8 ones8 = __builtin_bit_cast(bf16x8, (u32x4){ONES2, ONES2, ONES2, ONES2});
    bf16x8 wX[7], xX[2], wY[7], xY[2];
    int slot = 0;
#define Q_READ(W_, X_, ks) do { const lds_cptr sp_ = (lds_cptr)lds + slot * Q_SB; \
    X_[0] = ldsr128(sp_ + ((ks) ? xrd1 : xrd0)); X_[1] = ldsr128(sp_ + ((ks) ? xrd1 : xrd0) + 2048); \
    _Pragma("unroll") for (int n = 0; n < 7; ++n) W_[n] = cat8(trread(sp_ + wrd[n] + (ks) * 7168), trread(sp_ + wrd[n] + (ks) * 7168 + 1792)); } while (0)
#define Q_MMA(W_, X_) do { \
    sx[0] = MFMA16(ones8, X_[0], sx[0]); sx[1] = MFMA16(ones8, X_[1], sx[1]); \
    sq[0] = MFMA16(X_[0], X_[0], sq[0]); sq[1] = MFMA16(X_[1], X_[1], sq[1]); \
    _Pragma("unroll") for (int n = 0; n < 7; ++n) { acc[n][0] = MFMA16(W_[n], X_[0], acc[n][0]); acc[n][1] = MFMA16(W_[n], X_[1], acc[n][1]); \
      if (n < 2) csa[n] = MFMA16(W_[n], ones8, csa[n]); } } while (0)
#define Q_ILV() do { _Pragma("unroll") for (int i_ = 0; i_ < 16; ++i_) { SGB(0x8, 1); SGB(0x100, 1); } SGB(0x8, 4); } while (0)
#define TDELAY 16
    {
      __builtin_amdgcn_s_sleep(TDELAY);
      unsigned i = vw * 64u + lane, rw = i / 27u;
      const char* wq = (const char*)wqkv + 192 * 3456 + (xg & 1) * 1728;
      touch4(wq, rw * 3456u + (i - rw * 27u) * 64u, lds0 + DUMMY);
      if (vw < 96u) { rw = i / 12u; touch4((const char*)x + (size_t)(xg >> 1) * 589824 + 384, rw * 1152u + (i - rw * 12u) * 64u, lds0 + DUMMY); }
      else { i += 2048u; rw = i / 27u; touch4(wq, rw * 3456u + (i - rw * 27u) * 64u, lds0 + DUMMY); }
      if (vw < 2u) { i = 10240u + vw * 64u + lane; rw = i / 27u; touch4(wq, rw * 3456u + (i - rw * 27u) * 64u, lds0 + DUMMY); }
    }
    asm volatile("" :: "s"(ws));
    BARRIER();
    STAMP(0, 1);
    __builtin_amdgcn_s_waitcnt(0xC07F);
    Q_READ(wX, xX, 0);
    SB0();
#pragma unroll
    for (int t = 0; t < 9; ++t) {
      Q_READ(wY, xY, 1); Q_MMA(wX, xX); Q_ILV(); SB0();
      if (t < 8) {
        BARRIER(); slot = (slot + 1 == Q_ST) ? 0 : slot + 1;
        SB0();
        Q_READ(wX, xX, 0); Q_MMA(wY, xY); Q_ILV(); SB0();
      } else { Q_MMA(wY, xY); }
    }
    STAMP(0, 2);
    if (c16 == 0) { *(f32x4*)(csl + (2 * w) * 16 + g * 4) = csa[0]; if (w < 3) *(f32x4*)(csl + (2 * w + 1) * 16 + g * 4) = csa[1]; }
    float rstd[2], mr[2];
    const unsigned jm0 = eqmask(c16 & 3, 0), jm1 = eqmask(c16 & 3, 1), jm2 = eqmask(c16 & 3, 2), jm3 = eqmask(c16 & 3, 3);
#pragma unroll
    for (int tt = 0; tt < 2; ++tt) {
      const float dg = sel4(sq[tt][0], sq[tt][1], sq[tt][2], sq[tt][3], jm0, jm1, jm2, jm3);
      const float ss = __shfl(dg, (c16 >> 2) * 16 + c16);
      const float mean = sx[tt][0] * (1.f / 576.f);
      rstd[tt] = rsqrtf(fmaxf(ss * (1.f / 576.f) - mean * mean, 0.f) + LN_EPS);
      mr[tt] = -mean;
    }
    BARRIER();
#pragma unroll
    for (int n = 0; n < 7; ++n) {
      const int nt = Q_TILE(n);
      const f32x4 cs4 = *(const f32x4*)(csl + nt * 16 + g * 4);
#pragma unroll
      for (int e = 0; e < 2; ++e)
#pragma unroll
        for (int tt = 0; tt < 2; ++tt) {
          const float rs = rstd[tt] * qs[n][e];
          const float v0 = __builtin_fmaf(mr[tt], cs4[2 * e], acc[n][tt][2 * e]) * rs;
          const float v1 = __builtin_fmaf(mr[tt], cs4[2 * e + 1], acc[n][tt][2 * e + 1]) * rs;
          *(unsigned*)(lds + so[n][e] + tt * 16 * SP) = cvtpk(v0, v1);
        }
    }
    BARRIER();
  }
  STAMP(0, 3);
  {
    const unsigned t8 = (unsigned)tid & 255u;
    const unsigned Tl = t8 >> 1, c = t8 & 1u, T = (unsigned)bm * 128u + Tl;
    const unsigned oQK = (T >> 5) * 1024u + c * 512u + (T & 31u) * 16u;
    const unsigned T2 = (unsigned)bm * 128u + (t8 & 127u);
    const unsigned oKX = KXOFF + (T2 >> 7) * 512u + (T2 & 31u) * 16u + ((T2 >> 5) & 3u) * 4u;
    const unsigned vrec = t8 >> 5, vhh = (t8 >> 4) & 1u, vi = t8 & 15u;
    const lds_cptr vsrc = (lds_cptr)lds + ((vrec * 16u + vhh * 4u + (vi >> 2)) * SP + (vi & 3u) * 8u);
    const lds_cptr vsrx = (lds_cptr)lds + ((vrec * 16u + vhh * 4u + (vi >> 2)) * SP + 32u);
    const unsigned oV = 65536u + ((unsigned)bm * 8u + vrec) * VREC + vhh * 320u + vi * 16u;
#pragma unroll
    for (int i = 0; i < 3; ++i) {
      const int hl = (w8 >> 2) * 3 + i;
      const int hs = hg * 6 + hl, which = hs >> 5, h = hs & 31;
      char* base = ws + (which == 0 ? WS_QM + (size_t)h * 65536 : WS_KV + (size_t)h * KVHEAD);
      if (which == 2) {
        const bf16x8 vm = cat8(trread(vsrc + hl * 48), trread(vsrc + hl * 48 + 8 * SP));
        bf16x8 vx = cat8(trread(vsrx + hl * 48), trread(vsrx + hl * 48 + 8 * SP));
        st16(base + oV, __builtin_bit_cast(u32x4, vm));
        if (vi == 2u) vx = __builtin_bit_cast(bf16x8, (u32x4){ONES2, ONES2, ONES2, ONES2});
        if (vi == 3u) vx = (bf16x8){0, 0, 0, 0, 0, 0, 0, 0};
        if (vi < 4u) st16(base + oV + 256u, __builtin_bit_cast(u32x4, vx));
      } else {
        st16(base + oQK, *(const u32x4*)(lds + Tl * SP + hl * 48 + c * 16));
        if (t8 < 128u) {
          const unsigned xv = *(const unsigned*)(lds + t8 * SP + hl * 48 + 32);
          if (which == 0) st4(ws + WS_QX + (size_t)h * 8192 + T2 * 4u, xv);
          else st4(base + oKX, xv);
        }
      }
    }
  }
  if (tid >= 256 && tid - 256 < 162) {
    u32x4 v;
#pragma unroll
    for (int j = 0; j < 4; ++j) v[j] = (unsigned)wog[2 * j] | ((unsigned)wog[2 * j + 1] << 16);
    st16(ws + WS_WOF + (size_t)(b * 162 + tid - 256) * 16, v);
  }
  STAMP(0, 4);
  asm volatile("s_waitcnt vmcnt(0)" ::: "memory");
#undef Q_ISSUE
#undef Q_TILE
#undef Q_WAIT
#undef Q_READ
#undef Q_MMA
#undef Q_ILV
}

#define EX2(v) __builtin_amdgcn_exp2f(v)
__global__ __launch_bounds__(512, 1) void k2_attn(char* __restrict__ ws, const u16* __restrict__ w1, const u16* __restrict__ w2,
                                                   const u16* __restrict__ x) {
  __shared__ __attribute__((aligned(16))) char lds[KVHEAD + 1024];
  const int tid = threadIdx.x, lane = tid & 63;
  const int w = __builtin_amdgcn_readfirstlane(tid >> 6);
  const int b = blockIdx.x, xg = b & 7;
  const int h = xg * 4 + ((b >> 3) & 3), qt = b >> 5;
  const int qp = w & 3, half = w >> 2;
  const unsigned lds0 = (unsigned)(uintptr_t)lds;
  const int r = lane & 31, hh = lane >> 5;
  const char* kvg = ws + WS_KV + (size_t)h * KVHEAD;
  const unsigned l16 = (unsigned)lane * 16u;
  STAMP(1, 0);
  u32x4 qmA, qmB; unsigned qxA, qxB;
  {
    const char* qmp = ws + WS_QM + (size_t)h * 65536 + (size_t)(qt * 8 + qp * 2) * 1024 + lane * 16;
    const char* qxp = ws + WS_QX + (size_t)h * 8192 + (size_t)(qt * 256 + qp * 64 + r) * 4;
    asm volatile("global_load_dwordx4 %0, %1, off" : "=v"(qmA) : "v"(qmp) : "memory");
    asm volatile("global_load_dwordx4 %0, %1, off offset:1024" : "=v"(qmB) : "v"(qmp) : "memory");
    asm volatile("global_load_dword %0, %1, off" : "=v"(qxA) : "v"(qxp) : "memory");
    asm volatile("global_load_dword %0, %1, off offset:128" : "=v"(qxB) : "v"(qxp) : "memory");
  }
#pragma unroll
  for (int i = 0; i < 6; ++i) {
    const int s = i * 8 + w;
    const int p = (s < 10) ? s : (s < 20) ? 32 + s - 10 : (s < 30) ? 64 + s - 20 : (s < 40) ? 104 + s - 30 : 144 + s - 40;
    glds16(kvg + (size_t)p * 1024, l16, lds0 + p * 1024);
  }
#define STAGEB(i) do { const int s_ = (i) * 8 + w; \
    const int p_ = (s_ < 22) ? 10 + s_ : (s_ < 44) ? 42 + s_ - 22 : (s_ < 74) ? 74 + s_ - 44 : 114 + s_ - 74; \
    glds16(kvg + (size_t)p_ * 1024, l16, lds0 + p_ * 1024); } while (0)
  CODE_TOUCH(SYM_K2, SYM_K4, w, lds0 + KVHEAD);
  const lds_cptr vp = (lds_cptr)lds + (65536u + (unsigned)half * 64u * VREC + hh * 320 + (r < 19 ? r : 19) * 16);
  const lds_cptr kp = (lds_cptr)lds + (half * 32768 + hh * 512 + r * 16);
  const lds_cptr xp = (lds_cptr)lds + (KXOFF + half * 4096 + r * 16);

  const char* pfb = (const char*)w1 + (size_t)(PFK1 * 2048 + b * 8 + w) * 1024;
  asm volatile("s_waitcnt vmcnt(0)\n\ts_barrier" : "+v"(qmA), "+v"(qmB), "+v"(qxA), "+v"(qxB) :: "memory");
  STAMP(1, 1);
  const bf16x8 qA = __builtin_bit_cast(bf16x8, qmA), qB = __builtin_bit_cast(bf16x8, qmB);
  bf16x8 eA[4], eB[4];
  {
    const unsigned a = hh == 0 ? qxA : 0u, bq = hh == 0 ? qxB : 0u;
    eA[0] = __builtin_bit_cast(bf16x8, (u32x4){a, 0u, 0u, 0u}); eB[0] = __builtin_bit_cast(bf16x8, (u32x4){bq, 0u, 0u, 0u});
    eA[1] = __builtin_bit_cast(bf16x8, (u32x4){0u, a, 0u, 0u}); eB[1] = __builtin_bit_cast(bf16x8, (u32x4){0u, bq, 0u, 0u});
    eA[2] = __builtin_bit_cast(bf16x8, (u32x4){0u, 0u, a, 0u}); eB[2] = __builtin_bit_cast(bf16x8, (u32x4){0u, 0u, bq, 0u});
    eA[3] = __builtin_bit_cast(bf16x8, (u32x4){0u, 0u, 0u, a}); eB[3] = __builtin_bit_cast(bf16x8, (u32x4){0u, 0u, 0u, bq});
  }
  const f32x16 zero16 = {};
  f32x16 oA = {}, oB = {}, sA, sB;
  u32x4 pAl = {0u, 0u, 0u, 0u}, pAh = {0u, 0u, 0u, 0u}, pBl = {0u, 0u, 0u, 0u}, pBh = {0u, 0u, 0u, 0u};
  bf16x8 kf, kx, kxn, v0, v1 = {};
  kf = ldsr128(kp); kx = ldsr128(xp);
  sA = MFMA32(kf, qA, zero16); sA = MFMA32(kx, eA[0], sA);
  float e0 = 0.f, e1 = 0.f, e2 = 0.f, e3 = 0.f, f0, f1, f2, f3;
  SB0();
#define EX4(a, b_, c_, d_, S, i0) do { a = EX2(S[i0]); b_ = EX2(S[i0 + 1]); c_ = EX2(S[i0 + 2]); d_ = EX2(S[i0 + 3]); } while (0)
#define CV2(P, j0, a, b_, c_, d_) do { P[j0] = cvtpk(a, b_); P[j0 + 1] = cvtpk(c_, d_); } while (0)
#define PF8(x) __builtin_bit_cast(bf16x8, x)
#define PIN(x) asm volatile("" : "+v"(x))
#define PINE() asm volatile("" : "+v"(e0), "+v"(e1), "+v"(e2), "+v"(e3))
#define PINF() asm volatile("" : "+v"(f0), "+v"(f1), "+v"(f2), "+v"(f3))
#define EXA(a, b_, S, i0) do { a = EX2(S[i0]); b_ = EX2(S[i0 + 1]); asm volatile("" : "+v"(a), "+v"(b_)); SB0(); } while (0)
#define EXB(c_, d_, S, i0) do { c_ = EX2(S[i0 + 2]); d_ = EX2(S[i0 + 3]); } while (0)
#define TILE(t) do { \
    constexpr int ro_ = (t) * 2 * (int)VREC; \
      sB = MFMA32(kf, qB, zero16); PIN(sB); SB0(); v0 = ldsr128(vq + ro_); kf = ldsr128(kq + ((t) + 1) * 1024); \
           if ((t) == 3) { kxn = ldsr128(xq + 512); } \
           EX4(f0, f1, f2, f3, sA, 0); CV2(pBh, 2, e0, e1, e2, e3); PINF(); PIN(pBh); SB0(); \
      EXA(e0, e1, sA, 4); oB = MFMA32(v1, PF8(pBh), oB); PIN(oB); SB0(); v1 = ldsr128(vq + ro_ + VREC); \
           EXB(e2, e3, sA, 4); CV2(pAl, 0, f0, f1, f2, f3); PINE(); PIN(pAl); SB0(); \
      EXA(f0, f1, sA, 8); sB = MFMA32(kx, eB[t], sB); PIN(sB); SB0(); \
           EXB(f2, f3, sA, 8); CV2(pAl, 2, e0, e1, e2, e3); PINF(); PIN(pAl); SB0(); \
      EXA(e0, e1, sA, 12); __builtin_amdgcn_s_waitcnt(0xC07F); SB0(); oA = MFMA32(v0, PF8(pAl), oA); PIN(oA); SB0(); \
           EXB(e2, e3, sA, 12); CV2(pAh, 0, f0, f1, f2, f3); PINE(); PIN(pAh); SB0(); \
      EXA(f0, f1, sB, 0); sA = MFMA32(kf, qA, zero16); PIN(sA); SB0(); EXB(f2, f3, sB, 0); CV2(pAh, 2, e0, e1, e2, e3); PINF(); PIN(pAh); SB0(); \
      EXA(e0, e1, sB, 4); oA = MFMA32(v1, PF8(pAh), oA); PIN(oA); SB0(); EXB(e2, e3, sB, 4); CV2(pBl, 0, f0, f1, f2, f3); PINE(); PIN(pBl); SB0(); \
      EXA(f0, f1, sB, 8); sA = MFMA32((t) == 3 ? kxn : kx, eA[((t) + 1) & 3], sA); PIN(sA); SB0(); EXB(f2, f3, sB, 8); CV2(pBl, 2, e0, e1, e2, e3); PINF(); PIN(pBl); SB0(); \
      EXA(e0, e1, sB, 12); oB = MFMA32(v0, PF8(pBl), oB); PIN(oB); SB0(); EXB(e2, e3, sB, 12); CV2(pBh, 0, f0, f1, f2, f3); PINE(); PIN(pBh); SB0(); \
  } while (0)
#define SIDE(a) do { STAGEB(it * 8 + (a)); if (it * 8 + (a) + 1 < 13) STAGEB(it * 8 + (a) + 1); SB0(); } while (0)
#define PFETCH() do { glds16(pfb, l16, lds0 + KVHEAD); pfb += 2048 * 1024; if (++pfn == 9) pfb = (const char*)w2 + (size_t)(b * 8 + w) * 1024; SB0(); } while (0)
  int pfn = PFK1;
  lds_cptr vq = vp, kq = kp, xq = xp;
  const auto slabr = __builtin_amdgcn_make_buffer_rsrc(ws + WS_SLAB + (size_t)b * 40960, 0, 40960, 0x00020000);
  __builtin_amdgcn_s_setprio(3);
#pragma unroll 1
  for (int it = 0; it < 2; ++it) {
    SIDE(0); TILE(0);
    SIDE(2); TILE(1);
    SIDE(4);
    if (it == 1) { PFETCH(); }
    TILE(2);
    if (it == 0) SIDE(6); else { PFETCH(); }
    TILE(3);
    kx = kxn;
    vq += 8 * VREC; kq += 4096; xq += 512;
  }
  asm volatile("s_waitcnt vmcnt(2)\n\ts_barrier" ::: "memory");
  STAMP(1, 2);
  if (b < 8) { CODE_TOUCH(SYM_K4, SYM_END, w, lds0 + KVHEAD); }
#pragma unroll 1
  for (int it = 2; it < 7; ++it) {
    if (it == 4) __builtin_amdgcn_s_setprio(2);
    if (it == 6) __builtin_amdgcn_s_setprio(1);
    SB0();
    PFETCH(); TILE(0);
    if (it < 2 + (18 - PFK1 - PFTAIL - 12)) { PFETCH(); }
    TILE(1);
    PFETCH();
    TILE(2);
    __builtin_amdgcn_raw_buffer_store_b128((u32x4){~0u, ~0u, ~0u, ~0u}, slabr, (w * 5 + it - 2) * 1024 + lane * 16, 0, 16);
    SB0();
    TILE(3);
    kx = kxn;
    vq += 8 * VREC; kq += 4096; xq += 512;
  }
  const int tt32 = xg * 8 + (b >> 5), ct = (b >> 3) & 3;
  const int nt = ct * 9 + w, t3 = w & 1;
  const char* wfp = ws + WS_WOF + (size_t)nt * 18432 + lane * 16;
  bf16x8 bfr[18];
#define WOLD(a, n) do { _Pragma("unroll") for (int ks = (a); ks < (a) + (n); ++ks) bfr[ks] = *(const bf16x8*)(wfp + ks * 1024); SB0(); } while (0)
  __builtin_amdgcn_s_setprio(0);
  WOLD(0, 4); TILE(0);
  WOLD(4, 4); TILE(1);
  WOLD(8, 3); TILE(2);
  WOLD(11, 3); TILE(3);
  CV2(pBh, 2, e0, e1, e2, e3);
  oB = MFMA32(v1, PF8(pBh), oB);
  STAMP(1, 3);

  float mA = 0.f, mB = 0.f;
  {
    bool bad = false;
    if (hh == 0) {
      bad = !(oA[10] >= 8.271806125530277e-25f && oA[10] <= 1.2676506002282294e30f) ||
            !(oB[10] >= 8.271806125530277e-25f && oB[10] <= 1.2676506002282294e30f);
    }
#pragma unroll
    for (int i = 0; i < 10; ++i) bad |= !(__builtin_fabsf(oA[i]) <= 3.0e38f) || !(__builtin_fabsf(oB[i]) <= 3.0e38f);
    if (__any(bad)) {
#pragma unroll 1
      for (int qq = 0; qq < 2; ++qq) {
        const bf16x8 qf = qq ? qB : qA;
        f32x16 oacc = zero16;
        float m = -INFINITY;
        for (int it = 0; it < 8; ++it) {
          const bf16x8 kx_ = ldsr128(xp + it * 512);
#pragma unroll
          for (int t = 0; t < 4; ++t) {
            const bf16x8 kf_ = ldsr128(kp + (it * 4 + t) * 1024);
            f32x16 s_ = MFMA32(kf_, qf, zero16);
            s_ = MFMA32(kx_, qq ? eB[t] : eA[t], s_);
            float tm = s_[0];
#pragma unroll
            for (int i = 1; i < 16; ++i) tm = fmaxf(tm, s_[i]);
            tm = fmaxf(tm, __shfl_xor(tm, 32));
            const float mn = fmaxf(m, tm);
            const float sc = EX2(m - mn);
            m = mn;
#pragma unroll
            for (int i = 0; i < 16; ++i) oacc[i] *= sc;
            const int ro_ = (it * 8 + t * 2) * (int)VREC;
            const bf16x8 v0_ = ldsr128(vp + ro_), v1_ = ldsr128(vp + ro_ + VREC);
            u32x4 pa_, pb_;
#pragma unroll
            for (int i = 0; i < 4; ++i) {
              pa_[i] = cvtpk(EX2(s_[2 * i] - m), EX2(s_[2 * i + 1] - m));
              pb_[i] = cvtpk(EX2(s_[8 + 2 * i] - m), EX2(s_[8 + 2 * i + 1] - m));
            }
            oacc = MFMA32(v0_, PF8(pa_), oacc);
            oacc = MFMA32(v1_, PF8(pb_), oacc);
          }
        }
        if (qq) { oB = oacc; mB = m; } else { oA = oacc; mA = m; }
      }
      asm volatile("" ::: "memory");
      WOLD(0, 14);
    }
  }
  const int g = lane >> 4, c16 = lane & 15;
  constexpr int L_O = 32768, L_W8 = L_O + 36864, L_PST = L_W8 + 18432, L_FIN = L_PST + 2304;
  const unsigned hm0 = eqmask(half, 0), hm1 = eqmask(half, 1);
  BARRIER();
  {
    f32x4* mb = (f32x4*)(lds + w * 4096);
    float os[11];
#pragma unroll
    for (int i = 0; i < 11; ++i) os[i] = sel4(oA[i], oB[i], 0.f, 0.f, hm1, hm0, 0u, 0u);
    const float ms = sel4(mA, mB, 0.f, 0.f, hm1, hm0, 0u, 0u);
    mb[lane] = (f32x4){os[0], os[1], os[2], os[3]};
    mb[64 + lane] = (f32x4){os[4], os[5], os[6], os[7]};
    mb[128 + lane] = (f32x4){os[8], os[9], os[10], ms};
  }
  BARRIER();
  {
    const f32x4* mb = (const f32x4*)(lds + (w ^ 4) * 4096);
    float ok[11];
#pragma unroll
    for (int i = 0; i < 11; ++i) ok[i] = sel4(oA[i], oB[i], 0.f, 0.f, hm0, hm1, 0u, 0u);
    const float mk = sel4(mA, mB, 0.f, 0.f, hm0, hm1, 0u, 0u);
    const f32x4 a0 = mb[lane], a1 = mb[64 + lane], a2 = mb[128 + lane];
    const float M = fmaxf(mk, a2[3]);
    const float s0 = EX2(mk - M), s1 = EX2(a2[3] - M);
    const float lsum = __shfl(ok[10] * s0 + a2[2] * s1, r);
    const float inv = 1.0f / lsum;
    const float c0 = s0 * inv, c1 = s1 * inv;
#define NS(v) __builtin_elementwise_min((v), 0xFFFFFFFEu)
    u32x4 ov;
    ov[0] = NS(cvtpk(ok[0] * c0 + a0[0] * c1, ok[1] * c0 + a0[1] * c1));
    ov[1] = NS(cvtpk(ok[2] * c0 + a0[2] * c1, ok[3] * c0 + a0[3] * c1));
    ov[2] = NS(cvtpk(ok[4] * c0 + a1[0] * c1, ok[5] * c0 + a1[1] * c1));
    ov[3] = NS(cvtpk(ok[6] * c0 + a1[2] * c1, ok[7] * c0 + a1[3] * c1));
    const int T = qt * 256 + qp * 64 + half * 32 + r;
    char* ofb = ws + WS_OF + (size_t)(T >> 4) * 18432;
    st16(ofb + (h >> 1) * 1024 + (((h & 1) * 2 + hh) * 16 + (T & 15)) * 16, ov);
    if (hh == 0)
      st4(ofb + (16 + (h >> 4)) * 1024 + (((h & 15) >> 2) * 16 + (T & 15)) * 16 + (h & 3) * 4,
          NS(cvtpk(ok[8] * c0 + a2[0] * c1, ok[9] * c0 + a2[1] * c1)));
#undef NS
  }
  STAMP(1, 4);
  WOLD(14, 4);
  u32x2 xr[3];
  xr[0] = *(const u32x2*)(x + (size_t)(tt32 * 32 + c16) * 576 + nt * 16 + g * 4);
  xr[1] = *(const u32x2*)(x + (size_t)(tt32 * 32 + 16 + c16) * 576 + nt * 16 + g * 4);
  xr[2] = *(const u32x2*)(x + (size_t)(tt32 * 32 + t3 * 16 + c16) * 576 + (ct * 9 + 8) * 16 + g * 4);
#pragma unroll
  for (int i = 0; i < 3; ++i) {
    const int pc = (i * 8 + w < 18) ? i * 8 + w : 17;
    glds16(ws + WS_WOF + (size_t)(ct * 9 + 8) * 18432 + (size_t)pc * 1024, l16, lds0 + L_W8 + (unsigned)pc * 1024u);
  }
  asm volatile("s_waitcnt vmcnt(0)" ::: "memory");
  {
    const auto ofr = __builtin_amdgcn_make_buffer_rsrc(ws + WS_OF + (size_t)(tt32 * 2) * 18432, 0, 36864, 0x00020000);
    u32x4 op[5];
    for (unsigned spins = 0;; ++spins) {
      unsigned mx = 0;
#pragma unroll
      for (int i = 0; i < 5; ++i) op[i] = __builtin_amdgcn_raw_buffer_load_b128(ofr, ((i * 8 + w < 36) ? i * 8 + w : w) * 1024 + lane * 16, 0, 16);
#pragma unroll
      for (int i = 0; i < 5; ++i) mx = max(max(mx, max(op[i][0], op[i][1])), max(op[i][2], op[i][3]));
      if (__all(mx != 0xFFFFFFFFu) || spins > (1u << 20)) break;
      __builtin_amdgcn_s_sleep(2);
    }
#pragma unroll
    for (int i = 0; i < 5; ++i) *(u32x4*)(lds + L_O + ((i * 8 + w < 36) ? i * 8 + w : w) * 1024 + lane * 16) = op[i];
  }
  BARRIER();
  STAMP(1, 5);
#pragma unroll
  for (int i = 0; i < PFTAIL; ++i) PFETCH();
  f32x4 acc[3], acd[2];
  acc[0] = acc[1] = acc[2] = acd[0] = acd[1] = (f32x4){0, 0, 0, 0};
  const lds_cptr oa = (lds_cptr)lds + (L_O + lane * 16);
  if (w < 6) {
#pragma unroll
    for (int ks = 0; ks < 18; ks += 2) {
      acc[0] = MFMA16(bfr[ks], ldsr128(oa + ks * 1024), acc[0]);
      acc[1] = MFMA16(bfr[ks], ldsr128(oa + 18432 + ks * 1024), acc[1]);
      acd[0] = MFMA16(bfr[ks + 1], ldsr128(oa + (ks + 1) * 1024), acd[0]);
      acd[1] = MFMA16(bfr[ks + 1], ldsr128(oa + 18432 + (ks + 1) * 1024), acd[1]);
    }
    acc[0] += acd[0]; acc[1] += acd[1];
  } else {
    const lds_cptr o0 = oa + t3 * 18432, o1 = oa + (1 - t3) * 18432;
#pragma unroll
    for (int ks = 0; ks < 18; ++ks) {
      const bf16x8 of = ldsr128(o0 + ks * 1024);
      acd[0] = MFMA16(bfr[ks], of, acd[0]);
      acd[1] = MFMA16(bfr[ks], ldsr128(o1 + ks * 1024), acd[1]);
      acc[2] = MFMA16(ldsr128((lds_cptr)lds + (L_W8 + ks * 1024 + lane * 16)), of, acc[2]);
    }
    const unsigned tm0 = eqmask(t3, 0), tm1 = eqmask(t3, 1);
#pragma unroll
    for (int j = 0; j < 4; ++j) { acc[0][j] = sel4(acd[0][j], acd[1][j], 0.f, 0.f, tm0, tm1, 0u, 0u); acc[1][j] = sel4(acd[0][j], acd[1][j], 0.f, 0.f, tm1, tm0, 0u, 0u); }
  }
  float* hp = (float*)(ws + WS_H);
  float* pst = (float*)(lds + L_PST);
  float* fin = (float*)(lds + L_FIN);
#define G4SUM(v) do { auto s16_ = __builtin_amdgcn_permlane16_swap(__float_as_uint(v), __float_as_uint(v), false, false); \
    v = __uint_as_float(s16_[0]) + __uint_as_float(s16_[1]); \
    auto s32_ = __builtin_amdgcn_permlane32_swap(__float_as_uint(v), __float_as_uint(v), false, false); \
    v = __uint_as_float(s32_[0]) + __uint_as_float(s32_[1]); } while (0)
#pragma unroll
  for (int t = 0; t < 3; ++t) {
    acc[t] += (f32x4){__uint_as_float(xr[t][0] << 16), __uint_as_float(xr[t][0] & 0xffff0000u), __uint_as_float(xr[t][1] << 16), __uint_as_float(xr[t][1] & 0xffff0000u)};
    float sm = (acc[t][0] + acc[t][1]) + (acc[t][2] + acc[t][3]);
    G4SUM(sm);
    const float mu = sm * (1.f / 16.f);
    const float d0 = acc[t][0] - mu, d1 = acc[t][1] - mu, d2 = acc[t][2] - mu, d3 = acc[t][3] - mu;
    float m2 = (d0 * d0 + d1 * d1) + (d2 * d2 + d3 * d3);
    G4SUM(m2);
    if (g == 0 && (t < 2 || w >= 6)) {
      typedef float f32x2_t __attribute__((ext_vector_type(2)));
      *(f32x2_t*)(pst + ((t < 2) ? (w * 32 + t * 16 + c16) : (256 + t3 * 16 + c16)) * 2) = (f32x2_t){sm, m2};
    }
  }
#undef G4SUM
  BARRIER();
  STAMP(1, 6);
#define HSTORE() do { st16(hp + (size_t)((tt32 * 2) * 36 + nt) * 256 + lane * 4, __builtin_bit_cast(u32x4, acc[0])); \
    st16(hp + (size_t)((tt32 * 2 + 1) * 36 + nt) * 256 + lane * 4, __builtin_bit_cast(u32x4, acc[1])); \
    if (w >= 6) st16(hp + (size_t)((tt32 * 2 + t3) * 36 + ct * 9 + 8) * 256 + lane * 4, __builtin_bit_cast(u32x4, acc[2])); } while (0)
  if (w != 0) HSTORE();
  if (w == 0) {
    const int tok = lane & 31, st = lane >> 5;
    float sw[9], S = 0.f, M2 = 0.f;
#pragma unroll
    for (int i = 0; i < 9; ++i) { sw[i] = pst[(i * 32 + tok) * 2]; M2 += pst[(i * 32 + tok) * 2 + 1]; S += sw[i]; }
    const float mub = S * (1.f / 144.f);
#pragma unroll
    for (int i = 0; i < 9; ++i) { const float d = sw[i] * (1.f / 16.f) - mub; M2 += 16.f * d * d; }
    gu64* gr = (gu64*)(ws + WS_GRAN + (size_t)tt32 * 2048);
    __hip_atomic_store(gr + ct * 64 + lane, (1ull << 32) | (unsigned long long)__float_as_uint(st ? M2 : S), RLX_AGENT);
    float pv[4];
    for (unsigned spins = 0;; ++spins) {
      bool okk = true;
#pragma unroll
      for (int i = 0; i < 4; ++i) {
        const unsigned long long v = __hip_atomic_load(gr + i * 64 + lane, RLX_AGENT);
        pv[i] = __uint_as_float((unsigned)v);
        okk &= (v >> 32) == 1ull;
      }
      if (__all(okk) || spins > (1u << 22)) break;
      __builtin_amdgcn_s_sleep(1);
    }
    const float tot = (pv[0] + pv[1]) + (pv[2] + pv[3]);
    const float mu = tot * (1.f / 576.f);
    float extra = 0.f;
#pragma unroll
    for (int i = 0; i < 4; ++i) { const float d = pv[i] * (1.f / 144.f) - mu; extra += 144.f * d * d; }
    const float other = __shfl_xor(tot, 32);
    const float var = (other + extra) * (1.f / 576.f);
    const float r1 = rsqrtf(var + LN_EPS);
    const float r2 = rsqrtf(var * r1 * r1 + LN_EPS);
    if (st == 0) { fin[tok * 2] = mu; fin[tok * 2 + 1] = r1 * r2; }
  }
  BARRIER();
  STAMP(1, 7);
  {
    char* tb = lds + w * 4096;
#pragma unroll
    for (int t = 0; t < 3; ++t) {
      typedef float f32x2_t __attribute__((ext_vector_type(2)));
      const f32x2_t f = *(const f32x2_t*)(fin + (((t < 2) ? t : t3) * 16 + c16) * 2);
      *(u32x2*)(tb + (t * 16 + c16) * 40 + g * 8) =
          (u32x2){cvtpk((acc[t][0] - f[0]) * f[1], (acc[t][1] - f[0]) * f[1]), cvtpk((acc[t][2] - f[0]) * f[1], (acc[t][3] - f[0]) * f[1])};
    }
    const int q4 = c16 >> 2, p4 = c16 & 3;
    const lds_cptr tr = (lds_cptr)tb + ((8 * g + q4) * 40 + p4 * 8);
    const bf16x8 hf = cat8(trread(tr), trread(tr + 160));
    const lds_cptr tr3 = (lds_cptr)tb + ((32 + 8 * (g & 1) + q4) * 40 + p4 * 8);
    const bf16x8 hf3 = cat8(trread(tr3), trread(tr3 + 160));
    char* hnp = ws + WS_HN;
    st16(hnp + ((size_t)(nt * 64 + tt32) * 64 + lane) * 16, __builtin_bit_cast(u32x4, hf));
    if (w >= 6 && g < 2) st16(hnp + ((size_t)((ct * 9 + 8) * 64 + tt32) * 64 + t3 * 32 + lane) * 16, __builtin_bit_cast(u32x4, hf3));
  }
  if (w == 0) HSTORE();
  STAMP(1, 8);
  asm volatile("s_waitcnt vmcnt(0)" ::: "memory");
#undef HSTORE
#undef WOLD
#undef TILE
#undef SIDE
#undef PFETCH
#undef STAGEB
#undef EX4
#undef CV2
}

__device__ __forceinline__ float gelu_tanh(float v) {
  const float z = v * (0.7978845608028654f + 0.035677408136300125f * v * v);
  const float e = __builtin_amdgcn_exp2f(z * -2.8853900817779268f);
  return v * __builtin_amdgcn_rcpf(1.0f + e);
}
constexpr int G1_SB = 28672;
template <int MT>
__device__ __forceinline__ void g1_body(char* lds, const u16* __restrict__ w1, const u16* __restrict__ b1, char* __restrict__ ws,
                                        int lane, int w8, int dt0, int pn) {
  const int w = w8 & 3;
  const unsigned lds0 = (unsigned)(uintptr_t)lds;
  const int wr = w >> 1, wc = w & 1;
  const int g = lane >> 4, c16 = lane & 15;
  f32x4 acc[4][MT];
  if (w8 >= 4) {
    const char* hn = ws + WS_HN + (size_t)dt0 * 65536;
    unsigned aoff[MT];
#pragma unroll
    for (int i = 0; i < MT; ++i) { const int a = w + 4 * i; aoff[i] = (unsigned)((a >> 1) * 65536 + (a & 1) * 1024 + lane * 16); }
    const char* wb = (const char*)w1 + (size_t)((pn >> 2) * 8 + (pn & 3)) * 128;
    unsigned boff[4];
#pragma unroll
    for (int i = 0; i < 4; ++i) {
      const int row = (4 * w + i) * 4 + (lane >> 4), cp = lane & 15;
      const int f = (row & 3) | (((row >> 3) & 1) << 2);
      const int ch = (((cp >> 1) ^ f) << 1) | (cp & 1);
      boff[i] = (unsigned)(row * 9216 + (ch & 7) * 16 + (ch >> 3) * 512);
    }
#define G1_ISSUE(t) do { const unsigned sl_ = lds0 + (unsigned)((t) & 3) * G1_SB; \
    const char* ab_ = hn + (size_t)(t) * 2048; const char* bb_ = wb + (size_t)(t) * (64 * 9216); \
    _Pragma("unroll") for (int i = 0; i < 4; ++i) glds16(bb_, boff[i], sl_ + 12288u + (unsigned)(4 * w + i) * 1024u); \
    _Pragma("unroll") for (int i = 0; i < MT; ++i) glds16(ab_, aoff[i], sl_ + (unsigned)(w + 4 * i) * 1024u); } while (0)
#define G1_WAIT(n) asm volatile("s_waitcnt vmcnt(%0)\n\ts_barrier" :: "i"(n) : "memory")
    G1_ISSUE(0); G1_ISSUE(1); G1_ISSUE(2);
#pragma unroll 1
    for (int t = 0; t < 29; ++t) { G1_WAIT(2 * (4 + MT)); G1_ISSUE(t + 3); }
    G1_WAIT(2 * (4 + MT)); G1_WAIT(4 + MT); G1_WAIT(0);
    BARRIER();
    BARRIER();
#pragma unroll
    for (int n = 0; n < 2; ++n)
#pragma unroll
      for (int m = 0; m < MT; ++m) acc[n][m] = *(const f32x4*)(lds + ((w * 2 + n) * MT + m) * 1024 + lane * 16);
  } else {
    CODE_TOUCH(SYM_K4, SYM_K5, w + 4 * (dt0 & 1), lds0 + 4 * G1_SB);
    const int q4 = (lane & 15) >> 2, p4 = lane & 3;
    const int fl = q4 | ((g & 1) << 2);
    lds_cptr bp0[4], bp1[4];
#pragma unroll
    for (int n = 0; n < 4; ++n) {
      bp0[n] = (lds_cptr)lds + (12288 + (8 * g + q4) * 256 + (((wc * 4 + n) ^ fl) * 32) + 8 * p4);
      bp1[n] = bp0[n] + 2 * G1_SB;
      asm volatile("" : "+v"(bp1[n]));
    }
    const lds_cptr ap0 = (lds_cptr)lds + (wr * MT * 2048 + lane * 16);
    lds_cptr ap1 = ap0 + 2 * G1_SB;
    asm volatile("" : "+v"(ap1));
#pragma unroll
    for (int n = 0; n < 4; ++n)
#pragma unroll
      for (int m = 0; m < MT; ++m) acc[n][m] = (f32x4){0, 0, 0, 0};
    bf16x8 aX[MT], bX[4], aY[MT], bY[4];
#define G1_READ(A_, B_, sl, ks) do { const lds_cptr a_ = ((sl) < 2 ? ap0 : ap1) + ((sl) & 1) * G1_SB + (ks) * 1024; \
    _Pragma("unroll") for (int m = 0; m < MT; ++m) A_[m] = ldsr128(a_ + m * 2048); \
    _Pragma("unroll") for (int n = 0; n < 4; ++n) { const lds_cptr b_ = ((sl) < 2 ? bp0[n] : bp1[n]) + ((sl) & 1) * G1_SB + (ks) * 8192; \
      B_[n] = cat8(trread(b_), trread(b_ + 1024)); } } while (0)
#define G1_MMA(A_, B_) do { _Pragma("unroll") for (int n = 0; n < 4; ++n) _Pragma("unroll") for (int m = 0; m < MT; ++m) \
    acc[n][m] = MFMA16(B_[n], A_[m], acc[n][m]); } while (0)
#define G1_ILV() do { _Pragma("unroll") for (int i_ = 0; i_ < 6; ++i_) { SGB(0x8, 1); SGB(0x100, 2); } SGB(0x8, 4 * MT - 6); } while (0)
#define G1_STAGE(sl) do { \
    G1_READ(aY, bY, sl, 1); G1_MMA(aX, bX); G1_ILV(); SB0(); \
    BARRIER(); SB0(); \
    G1_READ(aX, bX, ((sl) + 1) & 3, 0); G1_MMA(aY, bY); G1_ILV(); SB0(); } while (0)
    BARRIER();
    STAMP(3, 1);
    G1_READ(aX, bX, 0, 0);
    SB0();
#pragma unroll 1
    for (int t = 0; t < 8; ++t) { G1_STAGE(0); G1_STAGE(1); G1_STAGE(2); G1_STAGE(3); }
    STAMP(3, 3);
    __builtin_amdgcn_s_waitcnt(0xC07F);
#pragma unroll
    for (int n = 0; n < 2; ++n)
#pragma unroll
      for (int m = 0; m < MT; ++m) *(f32x4*)(lds + ((w * 2 + n) * MT + m) * 1024 + lane * 16) = acc[2 + n][m];
    BARRIER();
  }
  char* up = ws + WS_U;
#pragma unroll
  for (int n = 0; n < 2; ++n) {
    const int ft = ((pn >> 2) * 8 + (pn & 3) + 4 * wc) * 4 + (w8 >> 2) * 2 + n;
    const u32x2 bv = *(const u32x2*)(b1 + ft * 16 + g * 4);
    const float bb0 = __uint_as_float(bv[0] << 16), bb1 = __uint_as_float(bv[0] & 0xffff0000u);
    const float bb2 = __uint_as_float(bv[1] << 16), bb3 = __uint_as_float(bv[1] & 0xffff0000u);
#pragma unroll
    for (int m = 0; m < MT; ++m) {
      const int dt = dt0 + wr * MT + m;
      u32x2 v;
      v[0] = cvtpk(gelu_tanh(acc[n][m][0] + bb0), gelu_tanh(acc[n][m][1] + bb1));
      v[1] = cvtpk(gelu_tanh(acc[n][m][2] + bb2), gelu_tanh(acc[n][m][3] + bb3));
      st8(up + ((size_t)(dt * 144 + (ft >> 1)) * 64 + ((ft & 1) * 2 + (g >> 1)) * 16 + c16) * 16 + (g & 1) * 8, v);
    }
  }
  STAMP(3, 4);
  asm volatile("s_waitcnt vmcnt(0)" ::: "memory");
#undef G1_ISSUE
#undef G1_WAIT
#undef G1_READ
#undef G1_MMA
#undef G1_ILV
#undef G1_STAGE
}
__global__ __launch_bounds__(512, 1) void k4_gemm1(const u16* __restrict__ w1, const u16* __restrict__ b1, char* __restrict__ ws) {
  __shared__ __attribute__((aligned(16))) char lds[4 * G1_SB + 256];
  const int tid = threadIdx.x, lane = tid & 63;
  const int w8 = __builtin_amdgcn_readfirstlane(tid >> 6);
  const int id = (blockIdx.x & 7) * 32 + (blockIdx.x >> 3);
  const int sid = id - (id >> 5) * 2 + ((id >> 5) * 3) / 8;
  const bool spare = (id & 31) >= 30 + ((((id >> 5) + 1) * 3) / 8 - ((id >> 5) * 3) / 8);
  if (spare) return;
  const int grp = sid / 27, r = sid - grp * 27;
  STAMP(3, 0);
  asm volatile("" :: "s"(b1), "s"(ws));
  if (r < 18) g1_body<3>(lds, w1, b1, ws, lane, w8, (r % 6) * 6, grp * 4 + r / 6);
  else        g1_body<2>(lds, w1, b1, ws, lane, w8, (r - 18) * 4, grp * 4 + 3);
}

constexpr int G2_ST = 4, G2_SB = 36864;
__global__ __launch_bounds__(512, 1) void k5_gemm2(const u16* __restrict__ w2, const u16* __restrict__ b2, char* __restrict__ ws,
                                                    u16* __restrict__ out) {
  __shared__ __attribute__((aligned(16))) char lds[G2_ST * G2_SB + 256];
  const int tid = threadIdx.x, lane = tid & 63;
  const int w8 = __builtin_amdgcn_readfirstlane(tid >> 6), w = w8 & 3;
  const int xg = blockIdx.x & 7, o = blockIdx.x >> 3;
  const int kq = xg & 3, pm = o & 3, pn = (xg >> 2) * 8 + (o >> 2);
  const int tile = pn * 4 + pm;
  const unsigned lds0 = (unsigned)(uintptr_t)lds;
  STAMP(4, 0);
  if (w8 >= 4) {
    const char* ub = ws + WS_U + (size_t)(pm * 9) * 147456 + (size_t)kq * 36864;
    unsigned aoff[5];
#pragma unroll
    for (int i = 0; i < 5; ++i) { int a = w + 4 * i; if (a >= 18) a -= 4; aoff[i] = (unsigned)((a >> 1) * 147456 + (a & 1) * 1024 + lane * 16); }
    const char* wb = (const char*)w2 + (size_t)kq * 1152 * 4096 + (size_t)((pn >> 2) * 8 + (pn & 3)) * 128;
    unsigned boff[4];
#pragma unroll
    for (int i = 0; i < 4; ++i) {
      const int row = (4 * w + i) * 4 + (lane >> 4), cp = lane & 15;
      const int f = (row & 3) | (((row >> 3) & 1) << 2);
      const int ch = (((cp >> 1) ^ f) << 1) | (cp & 1);
      boff[i] = (unsigned)(row * 4096 + (ch & 7) * 16 + (ch >> 3) * 512);
    }
#define G2_ISSUE(NA, t, slot) do { const unsigned sl_ = lds0 + (unsigned)(slot) * G2_SB; \
    const char* ab_ = ub + (size_t)(t) * 2048; const char* bb_ = wb + (size_t)(t) * (64 * 4096); \
    _Pragma("unroll") for (int i = 0; i < 4; ++i) glds16(bb_, boff[i], sl_ + 20480u + (unsigned)(4 * w + i) * 1024u); \
    _Pragma("unroll") for (int i = 0; i < NA; ++i) glds16(ab_, aoff[i], sl_ + (unsigned)(w + 4 * i) * 1024u); } while (0)
#define G2_LOADER(NA, W2_, W1_) do { \
    _Pragma("unroll") for (int t = 0; t < G2_ST - 1; ++t) G2_ISSUE(NA, t, t); \
    int islot = G2_ST - 1; \
    for (int t = 0; t < 18 - (G2_ST - 1); ++t) { \
      asm volatile("s_waitcnt vmcnt(" #W2_ ")\n\ts_barrier" ::: "memory"); \
      G2_ISSUE(NA, t + G2_ST - 1, islot); \
      islot = (islot + 1 == G2_ST) ? 0 : islot + 1; } \
    asm volatile("s_waitcnt vmcnt(" #W2_ ")\n\ts_barrier" ::: "memory"); \
    _Pragma("unroll") for (int i4 = 0; i4 < 5; ++i4) {         \
      const int tau_ = ((i4 == 4 && kq >= 2) ? 3 : i4) * 4 + kq; \
      const int st_ = ((pn >> 2) * 8 + (pn & 3) + 4 * (w >> 1)) * 4 + (w & 1) * 2 + (tau_ & 1); \
      glds16(ws + WS_H + (size_t)(st_ * 36 + pm * 9 + (tau_ >> 1)) * 1024, (unsigned)lane * 16u, lds0 + 2u * G2_SB + (unsigned)(w * 5 + i4) * 1024u); } \
    asm volatile("s_waitcnt vmcnt(" #W1_ ")\n\ts_barrier" ::: "memory"); \
    asm volatile("s_waitcnt vmcnt(0)\n\ts_barrier" ::: "memory"); } while (0)
    if (w < 2) G2_LOADER(5, 18, 14); else G2_LOADER(4, 16, 13);
    return;
  }
  CODE_TOUCH(SYM_K5, SYM_END, w, lds0 + G2_ST * G2_SB);
  const int g = lane >> 4, c16 = lane & 15, q4 = (lane & 15) >> 2, p4 = lane & 3;
  const int fl = q4 | ((g & 1) << 2);
  unsigned brd[2];
#pragma unroll
  for (int n = 0; n < 2; ++n) brd[n] = (unsigned)(20480 + (8 * g + q4) * 256 + (((w * 2 + n) ^ fl) * 32) + 8 * p4);
  const unsigned ard = (unsigned)(lane * 16);
  u16 bb[5]; unsigned oo[5];
#pragma unroll
  for (int i4 = 0; i4 < 5; ++i4) {
    const int ie = (i4 == 4 && kq >= 2) ? 3 : i4;
    const int tau = ie * 4 + kq, m = tau >> 1, n = tau & 1;
    const int sp_ = ((pn >> 2) * 8 + (pn & 3) + 4 * (w >> 1)) * 64 + ((w & 1) * 2 + n) * 16 + c16, d0 = pm * 144 + m * 16 + g * 4;
    oo[i4] = (unsigned)(sp_ * 576 + d0);
    bb[i4] = b2[sp_];
  }
  f32x4 acc[18];
#pragma unroll
  for (int i = 0; i < 18; ++i) acc[i] = (f32x4){0, 0, 0, 0};
  int slot = 0;
  bf16x8 aX[9], bX[2], aY[9], bY[2];
#define G2_READ(A_, B_, ks) do { const lds_cptr sp_ = (lds_cptr)lds + slot * G2_SB; \
    _Pragma("unroll") for (int n = 0; n < 2; ++n) B_[n] = cat8(trread(sp_ + brd[n] + (ks) * 8192), trread(sp_ + brd[n] + (ks) * 8192 + 1024)); \
    _Pragma("unroll") for (int m = 0; m < 9; ++m) A_[m] = ldsr128(sp_ + ard + m * 2048 + (ks) * 1024); } while (0)
#define G2_MMA(A_, B_) do { _Pragma("unroll") for (int m = 0; m < 9; ++m) { \
    acc[m * 2] = MFMA16(A_[m], B_[0], acc[m * 2]); acc[m * 2 + 1] = MFMA16(A_[m], B_[1], acc[m * 2 + 1]); } } while (0)
#define G2_ILV() do { _Pragma("unroll") for (int i_ = 0; i_ < 13; ++i_) { SGB(0x8, 1); SGB(0x100, 1); } SGB(0x8, 5); } while (0)
  asm volatile("" :: "s"(b2), "s"(ws), "s"(out));
  BARRIER();
  STAMP(4, 1);
  __builtin_amdgcn_s_waitcnt(0xC07F);
  G2_READ(aX, bX, 0);
  SB0();
  for (int t = 0; t < 17; ++t) {
    G2_READ(aY, bY, 1); G2_MMA(aX, bX); G2_ILV(); SB0();
    BARRIER();
    slot = (slot + 1 == G2_ST) ? 0 : slot + 1;
    SB0();
    G2_READ(aX, bX, 0); G2_MMA(aY, bY); G2_ILV(); SB0();
  }
  G2_READ(aY, bY, 1); G2_MMA(aX, bX); G2_ILV(); SB0();
  G2_MMA(aY, bY);
  STAMP(4, 3);
  const auto rsrc = __builtin_amdgcn_make_buffer_rsrc(ws + WS_SLAB + (size_t)tile * SLABT, 0, SLABT, 0x00020000);
#define PKW(i, e) __builtin_elementwise_min(cvtpk(acc[i][2 * (e)], acc[i][2 * (e) + 1]), 0xFFFFFFFEu)
#pragma unroll
  for (int dst = 0; dst < 4; ++dst) {
    if (dst != kq) {
      const int sb = (dst * 4 + kq) * 10240 + w * 2560;
      __builtin_amdgcn_raw_buffer_store_b128((u32x4){PKW(dst, 0), PKW(dst, 1), PKW(dst + 4, 0), PKW(dst + 4, 1)}, rsrc, sb + lane * 16, 0, 16);
      __builtin_amdgcn_raw_buffer_store_b128((u32x4){PKW(dst + 8, 0), PKW(dst + 8, 1), PKW(dst + 12, 0), PKW(dst + 12, 1)}, rsrc, sb + 1024 + lane * 16, 0, 16);
      if (dst < 2) __builtin_amdgcn_raw_buffer_store_b64((u32x2){PKW((dst + 16) % 18, 0), PKW((dst + 16) % 18, 1)}, rsrc, sb + 2048 + lane * 8, 0, 16);
    }
  }
#undef PKW
  STAMP(4, 4);
  f32x4 own[5];
  const unsigned km0 = eqmask(kq, 0), km1 = eqmask(kq, 1), km2 = eqmask(kq, 2), km3 = eqmask(kq, 3);
#pragma unroll
  for (int i4 = 0; i4 < 5; ++i4) {
#pragma unroll
    for (int j = 0; j < 4; ++j)
      own[i4][j] = sel4(acc[i4 * 4][j], acc[i4 * 4 + 1][j], acc[(i4 * 4 + 2) % 18][j], acc[(i4 * 4 + 3) % 18][j], km0, km1, km2, km3);
    own[i4] += *(const f32x4*)(lds + 2 * G2_SB + (w * 5 + i4) * 1024 + lane * 16) + bf2f(bb[i4]);
  }
  asm volatile("s_waitcnt vmcnt(0)" ::: "memory");
  u32x4 pa[3], pb[3]; u32x2 pc[3];
  const int x8 = (kq < 2) ? 2048 + lane * 8 : lane * 16;
  for (unsigned spins = 0;; ++spins) {
#pragma unroll
    for (int s = 0; s < 3; ++s) {
      const int sb = (kq * 4 + ((kq + s + 1) & 3)) * 10240 + w * 2560;
      pa[s] = __builtin_amdgcn_raw_buffer_load_b128(rsrc, sb + lane * 16, 0, 16);
      pb[s] = __builtin_amdgcn_raw_buffer_load_b128(rsrc, sb + 1024 + lane * 16, 0, 16);
      pc[s] = __builtin_amdgcn_raw_buffer_load_b64(rsrc, sb + x8, 0, 16);
    }
    unsigned mx = 0;
#pragma unroll
    for (int s = 0; s < 3; ++s) {
#pragma unroll
      for (int e = 0; e < 4; ++e) mx = max(mx, max(pa[s][e], pb[s][e]));
      mx = max(mx, max(pc[s][0], pc[s][1]));
    }
    if (__all(mx != 0xFFFFFFFFu) || spins > (1u << 20)) break;
    __builtin_amdgcn_s_sleep(2);
  }
  STAMP(4, 5);
#define UPK(d) (f32x4){__uint_as_float((d)[0] << 16), __uint_as_float((d)[0] & 0xffff0000u), __uint_as_float((d)[1] << 16), __uint_as_float((d)[1] & 0xffff0000u)}
#pragma unroll
  for (int i4 = 0; i4 < 5; ++i4) {
    f32x4 a = own[i4];
#pragma unroll
    for (int s = 0; s < 3; ++s) {
      const u32x2 d = (i4 == 0) ? (u32x2){pa[s][0], pa[s][1]} : (i4 == 1) ? (u32x2){pa[s][2], pa[s][3]} :
                      (i4 == 2) ? (u32x2){pb[s][0], pb[s][1]} : (i4 == 3) ? (u32x2){pb[s][2], pb[s][3]} : pc[s];
      a += UPK(d);
    }
    if (i4 < 4 || kq < 2) st8(out + oo[i4], (u32x2){cvtpk(a[0], a[1]), cvtpk(a[2], a[3])});
  }
#undef UPK
  STAMP(4, 6);
#undef G2_ISSUE
#undef G2_READ
#undef G2_MMA
#undef G2_LOADER
}

__global__ void k_end() {}

extern "C" void kernel_launch(void* const* d_in, const int* in_sizes, int n_in,
                              void* d_out, int out_size, void* d_ws, size_t ws_size,
                              hipStream_t stream) {
  const u16* x    = (const u16*)d_in[0];
  const u16* wqkv = (const u16*)d_in[1];
  const u16* wo   = (const u16*)d_in[2];
  const u16* w1   = (const u16*)d_in[4];
  const u16* b1   = (const u16*)d_in[5];
  const u16* w2   = (const u16*)d_in[6];
  const u16* b2   = (const u16*)d_in[7];
  char* ws = (char*)d_ws;
  k1_qkv<<<256, 512, 0, stream>>>(x, wqkv, wo, w1, ws);
  k2_attn<<<256, 512, 0, stream>>>(ws, w1, w2, x);
  k4_gemm1<<<256, 512, 0, stream>>>(w1, b1, ws);
  k5_gemm2<<<256, 512, 0, stream>>>(w2, b2, ws, (u16*)d_out);
}
```
